```python
import torch

def _softmax(x, axis=-1):
    return torch.softmax(x, dim=axis)
def _gelu(x):
    return torch.nn.functional.gelu(x, approximate="tanh")
def _silu(x):
    return torch.nn.functional.silu(x)
def _sigmoid(x):
    return torch.sigmoid(x)
def _layernorm(x, axis=-1, eps=1e-5):
    m = x.mean(dim=axis, keepdim=True)
    v = x.var(dim=axis, keepdim=True, unbiased=False)
    return (x - m) / torch.sqrt(v + eps)
def _scatter_add(tgt, idx, src, axis=0):
    return tgt.clone().index_add_(axis, idx, src)
def _segment_sum(src, seg, n):
    out = torch.zeros((n,) + src.shape[1:], dtype=src.dtype)
    return out.index_add_(0, seg, src)
def _pad(x, pw):
    flat = tuple(v for pair in reversed(pw) for v in pair)
    return torch.nn.functional.pad(x, flat)

kernel_name = 'concat_heads__gqa_block_cached__gqa_block_cached__near_ridge'

def setup_inputs():

    _w = (lambda a: (lambda t: t.to(torch.bfloat16) if t.is_floating_point() else t)(torch.as_tensor(a)))
    def _A_setup():
        B, Sq, D = 16, 96, 512
        Hq, Hkv, Sk, Dh = 32, 32, 96, 128
        assert Hq % Hkv == 0, (Hq, Hkv)
        return {
            "x": _w((lambda *s: torch.randn(*s, dtype=torch.bfloat16) * 0.8)(B, Sq, D)),
            "k_cache": _w((lambda *s: torch.randn(*s, dtype=torch.bfloat16) * 0.8)(B, Hkv, Sk, Dh)),
            "v_cache": _w((lambda *s: torch.randn(*s, dtype=torch.bfloat16) * 0.05)(B, Hkv, Sk, Dh)),
            "wq": _w((lambda *s: torch.randn(*s, dtype=torch.bfloat16) * 0.8)(D, Hq * Dh) * (D ** -0.5)),
            "wo": _w((lambda *s: torch.randn(*s, dtype=torch.bfloat16) * 0.05)(Hq * Dh, D) * ((Hq * Dh) ** -0.5)),
        }
    _A_in = _A_setup()

    def _B_setup():
        B, Sq, D = 16, 96, 512
        Hq, Hkv, Sk, Dh = 32, 32, 96, 128
        assert Hq % Hkv == 0, (Hq, Hkv)
        return {
            "x": _w((lambda *s: torch.randn(*s, dtype=torch.bfloat16) * 0.8)(B, Sq, D)),
            "k_cache": _w((lambda *s: torch.randn(*s, dtype=torch.bfloat16) * 0.8)(B, Hkv, Sk, Dh)),
            "v_cache": _w((lambda *s: torch.randn(*s, dtype=torch.bfloat16) * 0.05)(B, Hkv, Sk, Dh)),
            "wq": _w((lambda *s: torch.randn(*s, dtype=torch.bfloat16) * 0.8)(D, Hq * Dh) * (D ** -0.5)),
            "wo": _w((lambda *s: torch.randn(*s, dtype=torch.bfloat16) * 0.05)(Hq * Dh, D) * ((Hq * Dh) ** -0.5)),
        }
    _B_in = _B_setup()

    return {
        "A_x": _A_in["x"],
        "A_k_cache": _A_in["k_cache"],
        "A_v_cache": _A_in["v_cache"],
        "A_wq": _A_in["wq"],
        "A_wo": _A_in["wo"],
        "B_x": _B_in["x"],
        "B_k_cache": _B_in["k_cache"],
        "B_v_cache": _B_in["v_cache"],
        "B_wq": _B_in["wq"],
        "B_wo": _B_in["wo"],
    }

def reference(A_x, A_k_cache, A_v_cache, A_wq, A_wo, B_x, B_k_cache, B_v_cache, B_wq, B_wo):
    def _A_ref(x, k_cache, v_cache, wq, wo):
        B, Sq, D = x.shape
        Hq, Dh = 32, 128
        Hkv = k_cache.shape[1]
        rep = Hq // Hkv
        q = (lambda x, *d: x.permute(*d))((x @ wq).reshape(B, Sq, Hq, Dh), 0, 2, 1, 3)
        k = (lambda x, n, axis: torch.repeat_interleave(x, n, dim=axis))(k_cache, rep, axis=1)
        v = (lambda x, n, axis: torch.repeat_interleave(x, n, dim=axis))(v_cache, rep, axis=1)
        scores = torch.einsum("bhqd,bhkd->bhqk", q, k) * (Dh ** -0.5)
        p = _softmax(scores, axis=-1)
        o = torch.einsum("bhqk,bhkd->bhqd", p, v)
        return (lambda x, *d: x.permute(*d))(o, 0, 2, 1, 3).reshape(B, Sq, Hq * Dh) @ wo

    def _B_ref(x, k_cache, v_cache, wq, wo):
        B, Sq, D = x.shape
        Hq, Dh = 32, 128
        Hkv = k_cache.shape[1]
        rep = Hq // Hkv
        q = (lambda x, *d: x.permute(*d))((x @ wq).reshape(B, Sq, Hq, Dh), 0, 2, 1, 3)
        k = (lambda x, n, axis: torch.repeat_interleave(x, n, dim=axis))(k_cache, rep, axis=1)
        v = (lambda x, n, axis: torch.repeat_interleave(x, n, dim=axis))(v_cache, rep, axis=1)
        scores = torch.einsum("bhqd,bhkd->bhqk", q, k) * (Dh ** -0.5)
        p = _softmax(scores, axis=-1)
        o = torch.einsum("bhqk,bhkd->bhqd", p, v)
        return (lambda x, *d: x.permute(*d))(o, 0, 2, 1, 3).reshape(B, Sq, Hq * Dh) @ wo

    a = _A_ref(A_x, A_k_cache, A_v_cache, A_wq, A_wo)
    b = _B_ref(B_x, B_k_cache, B_v_cache, B_wq, B_wo)
    return torch.concatenate([a, b], axis=-1)
```

```cpp
#include <hip/hip_runtime.h>
#include <hip/hip_bf16.h>
#include <stdint.h>

typedef __attribute__((ext_vector_type(8))) short bf16x8;
typedef __attribute__((ext_vector_type(4))) short s16x4;
typedef __attribute__((ext_vector_type(4))) float f32x4;
typedef __attribute__((ext_vector_type(2))) float f32x2;
typedef __attribute__((ext_vector_type(4))) unsigned u32x4;
typedef __attribute__((ext_vector_type(2))) unsigned u32x2;
typedef __attribute__((ext_vector_type(2))) __bf16 bf16x2_t;
typedef __attribute__((address_space(3))) s16x4* lds_s16x4_ptr;

#define DI __device__ __forceinline__
#define MFMA16(a, b, c) __builtin_amdgcn_mfma_f32_16x16x32_bf16((a), (b), (c), 0, 0, 0)

namespace {

constexpr int NB = 16, SQ = 96, DM = 512, HQ = 32, DH = 128, HD = HQ * DH;

DI unsigned pack2(float a, float b) {
  f32x2 v = {a, b};
  return __builtin_bit_cast(unsigned, __builtin_convertvector(v, bf16x2_t));
}
DI s16x4 tr_read(unsigned lds_addr) {
  return __builtin_amdgcn_ds_read_tr16_b64_v4i16((lds_s16x4_ptr)(uintptr_t)lds_addr);
}
DI bf16x8 cat8(s16x4 lo, s16x4 hi) { return __builtin_shufflevector(lo, hi, 0, 1, 2, 3, 4, 5, 6, 7); }
DI float xg_max(float x) {
  auto s = __builtin_amdgcn_permlane16_swap(__float_as_uint(x), __float_as_uint(x), false, false);
  x = fmaxf(__uint_as_float(s[0]), __uint_as_float(s[1]));
  auto t = __builtin_amdgcn_permlane32_swap(__float_as_uint(x), __float_as_uint(x), false, false);
  return fmaxf(__uint_as_float(t[0]), __uint_as_float(t[1]));
}
DI float xg_sum(float x) {
  auto s = __builtin_amdgcn_permlane16_swap(__float_as_uint(x), __float_as_uint(x), false, false);
  x = __uint_as_float(s[0]) + __uint_as_float(s[1]);
  auto t = __builtin_amdgcn_permlane32_swap(__float_as_uint(x), __float_as_uint(x), false, false);
  return __uint_as_float(t[0]) + __uint_as_float(t[1]);
}

constexpr int K1_V_BYTES = 4 * SQ * 256;
constexpr int K1_W_BUF = 64 * 256;
constexpr int K1_LDS = K1_V_BYTES + 2 * K1_W_BUF;

__global__ __launch_bounds__(512, 2) void qattn_kernel(
    const short* __restrict__ xA, const short* __restrict__ kA, const short* __restrict__ vA,
    const short* __restrict__ wqA, const short* __restrict__ xB, const short* __restrict__ kB,
    const short* __restrict__ vB, const short* __restrict__ wqB, u32x4* __restrict__ o_ws) {
  extern __shared__ __attribute__((aligned(16))) char smem[];
  const int tid = threadIdx.x;
  const int lane = tid & 63;
  const int wave = __builtin_amdgcn_readfirstlane(tid >> 6);
  const int c = lane & 15, g = lane >> 4, qp = (lane & 15) >> 2, p = lane & 3;
  const int bid = blockIdx.x;
  const int xcd = bid & 7, idx = bid >> 3;
  const int blk = xcd >> 2;
  const int h = (xcd & 3) * 8 + (idx & 7);
  const int bg = idx >> 3;
  const int u = wave >> 1, qh = wave & 1;
  const int b = bg * 4 + u;

  const short* __restrict__ x = blk ? xB : xA;
  const short* __restrict__ kc = blk ? kB : kA;
  const short* __restrict__ vc = blk ? vB : vA;
  const short* __restrict__ wq = blk ? wqB : wqA;

  const unsigned lds0 = (unsigned)(uintptr_t)smem;
  const unsigned ldsW = lds0 + K1_V_BYTES;

  const int srow = tid >> 4, sch = tid & 15;
  const int wf0 = (srow & 3) | (((srow >> 3) & 1) << 2);
  const unsigned w_st_off = srow * 256 + ((((sch >> 1) ^ wf0) << 5) | ((sch & 1) << 4));
  const short* wq_src = wq + (size_t)srow * HD + h * DH + sch * 8;
  const unsigned v_st_off = srow * 256 + ((((sch >> 1) ^ (srow & 7)) << 5) | ((sch & 1) << 4));
  const short* v_src = vc + ((size_t)(bg * 4) * HQ + h) * (SQ * DH) + srow * DH + sch * 8;

  const int fl = qp | ((g & 1) << 2);
  unsigned wrd[8], vrd[8];
#pragma unroll
  for (int dt = 0; dt < 8; ++dt) {
    wrd[dt] = ldsW + (8 * g + qp) * 256 + 8 * p + ((dt ^ fl) << 5);
    vrd[dt] = lds0 + u * (SQ * 256) + (4 * g + qp) * 256 + 8 * p + ((dt ^ fl) << 5);
  }

  const short* xw = x + ((size_t)(b * SQ + qh * 48 + c)) * DM + 8 * g;

  f32x4 qacc[8][3];
#pragma unroll
  for (int dt = 0; dt < 8; ++dt)
#pragma unroll
    for (int t = 0; t < 3; ++t) qacc[dt][t] = (f32x4){0.f, 0.f, 0.f, 0.f};

  bf16x8 xf[2][2][3];
  u32x4 wst[2], vst[2];

#pragma unroll
  for (int i = 0; i < 2; ++i) wst[i] = *(const u32x4*)(wq_src + (size_t)(32 * i) * HD);
#pragma unroll
  for (int kk = 0; kk < 2; ++kk)
#pragma unroll
    for (int t = 0; t < 3; ++t) xf[0][kk][t] = *(const bf16x8*)(xw + t * 16 * DM + kk * 32);
#pragma unroll
  for (int i = 0; i < 2; ++i) *(u32x4*)(smem + K1_V_BYTES + w_st_off + i * 32 * 256) = wst[i];
#pragma unroll
  for (int i = 0; i < 2; ++i) wst[i] = *(const u32x4*)(wq_src + (size_t)(64 + 32 * i) * HD);

#pragma unroll
  for (int ci = 0; ci < 8; ++ci) {
    __syncthreads();
    if (ci + 1 < 8) {
#pragma unroll
      for (int i = 0; i < 2; ++i)
        *(u32x4*)(smem + K1_V_BYTES + ((ci + 1) & 1) * K1_W_BUF + w_st_off + i * 32 * 256) = wst[i];
    }
    if (ci >= 1 && ci <= 6) {
#pragma unroll
      for (int i = 0; i < 2; ++i) {
        const int j = 2 * (ci - 1) + i;
        *(u32x4*)(smem + (j / 3) * (SQ * 256) + (j % 3) * 32 * 256 + v_st_off) = vst[i];
      }
    }
    if (ci + 2 < 8) {
#pragma unroll
      for (int i = 0; i < 2; ++i) wst[i] = *(const u32x4*)(wq_src + (size_t)(64 * (ci + 2) + 32 * i) * HD);
    }
    if (ci + 1 < 8) {
#pragma unroll
      for (int kk = 0; kk < 2; ++kk)
#pragma unroll
        for (int t = 0; t < 3; ++t)
          xf[(ci + 1) & 1][kk][t] = *(const bf16x8*)(xw + t * 16 * DM + (ci + 1) * 64 + kk * 32);
    }
    if (ci <= 5) {
#pragma unroll
      for (int i = 0; i < 2; ++i) {
        const int j = 2 * ci + i;
        vst[i] = *(const u32x4*)(v_src + (size_t)(j / 3) * (HQ * SQ * DH) + (j % 3) * 32 * DH);
      }
    }
#pragma unroll
    for (int kk = 0; kk < 2; ++kk) {
#pragma unroll
      for (int dt = 0; dt < 8; ++dt) {
        const unsigned a0 = wrd[dt] + (ci & 1) * K1_W_BUF + kk * 32 * 256;
        const bf16x8 af = cat8(tr_read(a0), tr_read(a0 + 4 * 256));
#pragma unroll
        for (int t = 0; t < 3; ++t) qacc[dt][t] = MFMA16(af, xf[ci & 1][kk][t], qacc[dt][t]);
      }
    }
  }

  bf16x8 qf[4][3];
#pragma unroll
  for (int s = 0; s < 4; ++s)
#pragma unroll
    for (int t = 0; t < 3; ++t) {
      u32x4 r;
#pragma unroll
      for (int e = 0; e < 2; ++e) {
        const unsigned a = pack2(qacc[2 * s][t][2 * e], qacc[2 * s][t][2 * e + 1]);
        const unsigned bb = pack2(qacc[2 * s + 1][t][2 * e], qacc[2 * s + 1][t][2 * e + 1]);
        auto r1 = __builtin_amdgcn_permlane32_swap(a, bb, false, false);
        auto r2 = __builtin_amdgcn_permlane16_swap(r1[0], r1[1], false, false);
        r[e] = r2[0];
        r[2 + e] = r2[1];
      }
      qf[s][t] = __builtin_bit_cast(bf16x8, r);
    }

  const short* kw = kc + (((size_t)b * HQ + h) * SQ + c) * DH + 8 * g;
  f32x4 sacc[6][3];
#pragma unroll
  for (int kt = 0; kt < 6; ++kt)
#pragma unroll
    for (int t = 0; t < 3; ++t) sacc[kt][t] = (f32x4){0.f, 0.f, 0.f, 0.f};
#pragma unroll
  for (int kt = 0; kt < 6; ++kt) {
#pragma unroll
    for (int s = 0; s < 4; ++s) {
      const bf16x8 kf = *(const bf16x8*)(kw + kt * 16 * DH + s * 32);
#pragma unroll
      for (int t = 0; t < 3; ++t) sacc[kt][t] = MFMA16(kf, qf[s][t], sacc[kt][t]);
    }
  }

  const float cexp = 0.08838834764831845f * 1.4426950408889634f;
  bf16x8 pf[3][3];
  float rinv[3];
#pragma unroll
  for (int t = 0; t < 3; ++t) {
    float m = sacc[0][t][0];
#pragma unroll
    for (int kt = 0; kt < 6; ++kt)
#pragma unroll
      for (int r = 0; r < 4; ++r) m = fmaxf(m, sacc[kt][t][r]);
    m = xg_max(m);
    const float mc = m * cexp;
    float sum = 0.f;
#pragma unroll
    for (int kt = 0; kt < 6; ++kt)
#pragma unroll
      for (int r = 0; r < 4; ++r) {
        const float e = __builtin_amdgcn_exp2f(fmaf(sacc[kt][t][r], cexp, -mc));
        sacc[kt][t][r] = e;
        sum += e;
      }
    sum = xg_sum(sum);
    rinv[t] = 1.0f / sum;
#pragma unroll
    for (int s = 0; s < 3; ++s) {
      u32x4 r;
      r[0] = pack2(sacc[2 * s][t][0], sacc[2 * s][t][1]);
      r[1] = pack2(sacc[2 * s][t][2], sacc[2 * s][t][3]);
      r[2] = pack2(sacc[2 * s + 1][t][0], sacc[2 * s + 1][t][1]);
      r[3] = pack2(sacc[2 * s + 1][t][2], sacc[2 * s + 1][t][3]);
      pf[s][t] = __builtin_bit_cast(bf16x8, r);
    }
  }

  f32x4 oacc[8][3];
#pragma unroll
  for (int dt = 0; dt < 8; ++dt)
#pragma unroll
    for (int t = 0; t < 3; ++t) oacc[dt][t] = (f32x4){0.f, 0.f, 0.f, 0.f};
#pragma unroll
  for (int dt = 0; dt < 8; ++dt) {
#pragma unroll
    for (int s = 0; s < 3; ++s) {
      const unsigned a0 = vrd[dt] + s * 32 * 256;
      const bf16x8 af = cat8(tr_read(a0), tr_read(a0 + 16 * 256));
#pragma unroll
      for (int t = 0; t < 3; ++t) oacc[dt][t] = MFMA16(af, pf[s][t], oacc[dt][t]);
    }
  }

  u32x4* ow = o_ws + ((((size_t)(blk * NB + b) * HQ + h) * 4) * 6 + qh * 3) * 64 + lane;
#pragma unroll
  for (int s = 0; s < 4; ++s)
#pragma unroll
    for (int t = 0; t < 3; ++t) {
      const float ri = rinv[t];
      u32x4 r;
      r[0] = pack2(oacc[2 * s][t][0] * ri, oacc[2 * s][t][1] * ri);
      r[1] = pack2(oacc[2 * s][t][2] * ri, oacc[2 * s][t][3] * ri);
      r[2] = pack2(oacc[2 * s + 1][t][0] * ri, oacc[2 * s + 1][t][1] * ri);
      r[3] = pack2(oacc[2 * s + 1][t][2] * ri, oacc[2 * s + 1][t][3] * ri);
      ow[(s * 6 + t) * 64] = r;
    }
}

constexpr int K2_LDS = 65536;

__global__ __launch_bounds__(512, 2) void oproj_kernel(const u32x4* __restrict__ o_ws,
                                                       const short* __restrict__ woA,
                                                       const short* __restrict__ woB,
                                                       unsigned short* __restrict__ out) {
  extern __shared__ __attribute__((aligned(16))) char smem[];
  const int tid = threadIdx.x;
  const int lane = tid & 63;
  const int wave = __builtin_amdgcn_readfirstlane(tid >> 6);
  const int c = lane & 15, g = lane >> 4, qp = (lane & 15) >> 2, p = lane & 3;
  const int bid = blockIdx.x;
  const int xcd = bid & 7, idx = bid >> 3;
  const int blk = xcd >> 2;
  const int b = (xcd & 3) * 4 + (idx >> 3);
  const int nt = idx & 7;
  const int n0 = nt * 64;
  const short* __restrict__ wo = blk ? woB : woA;

  const unsigned lds0 = (unsigned)(uintptr_t)smem;
  char* wbuf = smem + wave * 8192;
  const unsigned ldsW = lds0 + wave * 8192;

  const int srow = lane >> 3, sch = lane & 7;
  const unsigned st_off = srow * 128 + ((((sch >> 1) ^ ((srow >> 1) & 3)) << 5) | ((sch & 1) << 4));
  const short* wsrc = wo + (size_t)(wave * 512 + srow) * DM + n0 + sch * 8;
  const int fl = (2 * g + (qp >> 1)) & 3;
  unsigned wrd[4];
#pragma unroll
  for (int j = 0; j < 4; ++j) wrd[j] = ldsW + (4 * g + qp) * 128 + 8 * p + ((j ^ fl) << 5);

  const u32x4* osrc = o_ws + (((size_t)(blk * NB + b) * HQ + wave * 4) * 4) * 6 * 64 + lane;

  f32x4 acc[4][6];
#pragma unroll
  for (int j = 0; j < 4; ++j)
#pragma unroll
    for (int t = 0; t < 6; ++t) acc[j][t] = (f32x4){0.f, 0.f, 0.f, 0.f};

  u32x4 wst[4];
  u32x4 of[2][6];
#pragma unroll
  for (int i = 0; i < 4; ++i) wst[i] = *(const u32x4*)(wsrc + (size_t)(8 * i) * DM);
#pragma unroll
  for (int t = 0; t < 6; ++t) of[0][t] = osrc[t * 64];
#pragma unroll
  for (int i = 0; i < 4; ++i) *(u32x4*)(wbuf + st_off + i * 8 * 128) = wst[i];
#pragma unroll
  for (int i = 0; i < 4; ++i) wst[i] = *(const u32x4*)(wsrc + (size_t)(32 + 8 * i) * DM);

#pragma unroll 2
  for (int ks = 0; ks < 16; ++ks) {
    if (ks + 1 < 16) {
#pragma unroll
      for (int i = 0; i < 4; ++i) *(u32x4*)(wbuf + ((ks + 1) & 1) * 4096 + st_off + i * 8 * 128) = wst[i];
    }
    if (ks + 2 < 16) {
#pragma unroll
      for (int i = 0; i < 4; ++i) wst[i] = *(const u32x4*)(wsrc + (size_t)(32 * (ks + 2) + 8 * i) * DM);
    }
    if (ks + 1 < 16) {
#pragma unroll
      for (int t = 0; t < 6; ++t) of[(ks + 1) & 1][t] = osrc[((ks + 1) * 6 + t) * 64];
    }
#pragma unroll
    for (int j = 0; j < 4; ++j) {
      const unsigned a0 = wrd[j] + (ks & 1) * 4096;
      const bf16x8 af = cat8(tr_read(a0), tr_read(a0 + 16 * 128));
#pragma unroll
      for (int t = 0; t < 6; ++t) acc[j][t] = MFMA16(af, __builtin_bit_cast(bf16x8, of[ks & 1][t]), acc[j][t]);
    }
  }

  f32x4* red = (f32x4*)smem;
#pragma unroll
  for (int rd = 0; rd < 3; ++rd) {
    __syncthreads();
#pragma unroll
    for (int j = 0; j < 4; ++j)
#pragma unroll
      for (int tt = 0; tt < 2; ++tt) red[(wave * 8 + j * 2 + tt) * 64 + lane] = acc[j][rd * 2 + tt];
    __syncthreads();
    f32x4 s = red[tid];
#pragma unroll
    for (int w = 1; w < 8; ++w) s += red[w * 512 + tid];
    const int j = wave >> 1, tt = wave & 1;
    const int q = (rd * 2 + tt) * 16 + c;
    const int n = n0 + j * 16 + 4 * g;
    u32x2 r;
    r[0] = pack2(s[0], s[1]);
    r[1] = pack2(s[2], s[3]);
    *(u32x2*)(out + (size_t)(b * SQ + q) * 1024 + blk * 512 + n) = r;
  }
}

}

extern "C" void kernel_launch(void* const* d_in, const int* in_sizes, int n_in, void* d_out, int out_size,
                              void* d_ws, size_t ws_size, hipStream_t stream) {
  const short* xA = (const short*)d_in[0];
  const short* kA = (const short*)d_in[1];
  const short* vA = (const short*)d_in[2];
  const short* wqA = (const short*)d_in[3];
  const short* woA = (const short*)d_in[4];
  const short* xB = (const short*)d_in[5];
  const short* kB = (const short*)d_in[6];
  const short* vB = (const short*)d_in[7];
  const short* wqB = (const short*)d_in[8];
  const short* woB = (const short*)d_in[9];
  u32x4* o_ws = (u32x4*)d_ws;
  (void)hipFuncSetAttribute((const void*)qattn_kernel, hipFuncAttributeMaxDynamicSharedMemorySize, K1_LDS);
  (void)hipFuncSetAttribute((const void*)oproj_kernel, hipFuncAttributeMaxDynamicSharedMemorySize, K2_LDS);
  hipLaunchKernelGGL(qattn_kernel, dim3(256), dim3(512), K1_LDS, stream, xA, kA, vA, wqA, xB, kB, vB, wqB, o_ws);
  hipLaunchKernelGGL(oproj_kernel, dim3(256), dim3(512), K2_LDS, stream, (const u32x4*)o_ws, woA, woB,
                     (unsigned short*)d_out);
}
```

```cpp
#include <hip/hip_runtime.h>
#include <hip/hip_bf16.h>
#include <stdint.h>

typedef __attribute__((ext_vector_type(8))) short bf16x8;
typedef __attribute__((ext_vector_type(4))) short s16x4;
typedef __attribute__((ext_vector_type(4))) float f32x4;
typedef __attribute__((ext_vector_type(2))) float f32x2;
typedef __attribute__((ext_vector_type(4))) unsigned u32x4;
typedef __attribute__((ext_vector_type(2))) unsigned u32x2;
typedef __attribute__((ext_vector_type(2))) __bf16 bf16x2_t;
typedef __attribute__((address_space(3))) s16x4* lds_s16x4_ptr;

#define DI __device__ __forceinline__
#define MFMA16(a, b, c) __builtin_amdgcn_mfma_f32_16x16x32_bf16((a), (b), (c), 0, 0, 0)

#define STAMP(i)
namespace {

constexpr int NB = 16, SQ = 96, DM = 512, HQ = 32, DH = 128, HD = HQ * DH;

DI unsigned pack2(float a, float b) {
  f32x2 v = {a, b};
  return __builtin_bit_cast(unsigned, __builtin_convertvector(v, bf16x2_t));
}
DI s16x4 tr_read(unsigned lds_addr) {
  return __builtin_amdgcn_ds_read_tr16_b64_v4i16((lds_s16x4_ptr)(uintptr_t)lds_addr);
}
DI bf16x8 cat8(s16x4 lo, s16x4 hi) { return __builtin_shufflevector(lo, hi, 0, 1, 2, 3, 4, 5, 6, 7); }
DI float xg_max(float x) {
  auto s = __builtin_amdgcn_permlane16_swap(__float_as_uint(x), __float_as_uint(x), false, false);
  x = fmaxf(__uint_as_float(s[0]), __uint_as_float(s[1]));
  auto t = __builtin_amdgcn_permlane32_swap(__float_as_uint(x), __float_as_uint(x), false, false);
  return fmaxf(__uint_as_float(t[0]), __uint_as_float(t[1]));
}
DI float xg_sum(float x) {
  auto s = __builtin_amdgcn_permlane16_swap(__float_as_uint(x), __float_as_uint(x), false, false);
  x = __uint_as_float(s[0]) + __uint_as_float(s[1]);
  auto t = __builtin_amdgcn_permlane32_swap(__float_as_uint(x), __float_as_uint(x), false, false);
  return __uint_as_float(t[0]) + __uint_as_float(t[1]);
}

constexpr int K1_LDS = 160 * 1024;
constexpr int K1_XBASE = 48 * 1024;
#define KV_NT 1

constexpr int n_w_at(int s) { return (s + 3 <= 15) ? 4 : 0; }
constexpr int n_x_at(int s) { return ((s & 1) && ((s + 3) / 2 <= 7)) ? 6 : 0; }
constexpr int issued_through(int s) {
  int n = 24;
  for (int j = 0; j <= s; ++j) n += n_w_at(j) + n_x_at(j);
  return n;
}
constexpr int pos_w(int st) {
  return st == 0 ? 4 : st == 1 ? 14 : st == 2 ? 18 : issued_through(st - 4) + 4;
}
constexpr int pos_x(int j) { return j == 0 ? 10 : j == 1 ? 24 : issued_through(2 * j - 3); }
constexpr int cmax(int a, int b) { return a > b ? a : b; }
constexpr int wait_before_barrier(int s) {
  return issued_through(s - 1) - cmax(pos_w(s + 1), pos_x((s + 1) >> 1));
}

template <int N> DI void wait_vm() { asm volatile("s_waitcnt vmcnt(%0)" ::"n"(N) : "memory"); }
DI void raw_barrier() {
  asm volatile("s_waitcnt lgkmcnt(0)" ::: "memory");
  __builtin_amdgcn_s_barrier();
  asm volatile("" ::: "memory");
}
DI void dma16(unsigned voff, const void* sbase, unsigned lds_dst) {
  unsigned keep;
  asm volatile("s_mov_b32 %0, m0\n\ts_mov_b32 m0, %3\n\ts_nop 0\n\tglobal_load_lds_dwordx4 %1, %2\n\ts_mov_b32 m0, %0"
               : "=&s"(keep) : "v"(voff), "s"(sbase), "s"(lds_dst) : "memory");
}
DI void dma16_nt(unsigned voff, const void* sbase, unsigned lds_dst) {
  unsigned keep;
  asm volatile("s_mov_b32 %0, m0\n\ts_mov_b32 m0, %3\n\ts_nop 0\n\tglobal_load_lds_dwordx4 %1, %2 nt\n\ts_mov_b32 m0, %0"
               : "=&s"(keep) : "v"(voff), "s"(sbase), "s"(lds_dst) : "memory");
}
DI void dma4(unsigned voff, const void* sbase, unsigned lds_dst) {
  unsigned keep;
  asm volatile("s_mov_b32 %0, m0\n\ts_mov_b32 m0, %3\n\ts_nop 0\n\tglobal_load_lds_dword %1, %2\n\ts_mov_b32 m0, %0"
               : "=&s"(keep) : "v"(voff), "s"(sbase), "s"(lds_dst) : "memory");
}
DI void mfma_acc(f32x4& c, bf16x8 a, bf16x8 b) {
  asm volatile("v_mfma_f32_16x16x32_bf16 %0, %1, %2, %0" : "+v"(c) : "v"(a), "v"(b));
}
DI void mfma_init(f32x4& c, bf16x8 a, bf16x8 b) {
  asm volatile("v_mfma_f32_16x16x32_bf16 %0, %1, %2, 0" : "=&v"(c) : "v"(a), "v"(b));
}
template <int V> struct IC { static constexpr int value = V; };
typedef const __attribute__((address_space(3))) bf16x8* lds_frag_ptr;
DI bf16x8 lds_frag(unsigned a) { return *(lds_frag_ptr)(uintptr_t)a; }

__global__ __launch_bounds__(512, 2) void qattn_kernel(
    const short* __restrict__ xA, const short* __restrict__ kA, const short* __restrict__ vA,
    const short* __restrict__ wqA, const short* __restrict__ woA, const short* __restrict__ xB,
    const short* __restrict__ kB, const short* __restrict__ vB, const short* __restrict__ wqB,
    const short* __restrict__ woB, u32x4* __restrict__ o_ws) {
  extern __shared__ __attribute__((aligned(16))) char smem[];
  const int tid = threadIdx.x;
  const int lane = tid & 63;
  const int wave = __builtin_amdgcn_readfirstlane(tid >> 6);
  const int c = lane & 15, g = lane >> 4, qp = (lane & 15) >> 2, p = lane & 3;
  const int bid = blockIdx.x;
  const int xcd = bid & 7, idx = bid >> 3;
  const int blk = xcd >> 2;
  const int h0 = ((xcd & 3) * 4 + (idx & 3)) * 2;
  const int b0 = (idx >> 2) * 2;
  const int u = wave >> 1, qh = wave & 1;
  const int bb = u >> 1, hh = u & 1;
  const int b = b0 + bb, h = h0 + hh;
  const bool wx_role = wave < 4;
  const int wr = wave & 3;

  const char* __restrict__ x = (const char*)(blk ? xB : xA) + (size_t)b0 * SQ * DM * 2;
  const char* __restrict__ kc = (const char*)(blk ? kB : kA) + ((size_t)b0 * HQ + h0) * (SQ * DH * 2);
  const char* __restrict__ vc = (const char*)(blk ? vB : vA) + ((size_t)b0 * HQ + h0) * (SQ * DH * 2);
  const char* __restrict__ wq = (const char*)(blk ? wqB : wqA) + h0 * DH * 2;
  const char* __restrict__ wo = (const char*)(blk ? woB : woA);

  const unsigned lds0 = (unsigned)(uintptr_t)smem;
  STAMP(0);

  unsigned voff[4];
  if (wx_role) {
#pragma unroll
    for (int e = 0; e < 2; ++e) {
      const int f = g | (e << 2);
      const int ch = (((c >> 1) ^ f) << 1) | (c & 1);
      voff[e] = (unsigned)((16 * (wr & 1) + g) * (HD * 2) + (wr >> 1) * (DH * 2) + ch * 16);
      const int chx = (lane & 7) ^ ((e << 2) | g);
      voff[2 + e] = (unsigned)((lane >> 3) * (DM * 2) + chx * 16);
    }
  } else {
#pragma unroll
    for (int e = 0; e < 2; ++e) {
      const int row = 4 * (2 * wr + e) + g;
      voff[e] = (unsigned)(row * 256 + ((c ^ (row & 15)) << 4));
      voff[2 + e] = (unsigned)(row * 256 + ((((((c >> 1) ^ (row & 7)) << 1) | (c & 1))) << 4));
    }
  }

  auto issue_w = [&](int st) {
#pragma unroll
    for (int e = 0; e < 4; ++e)
      dma16(voff[e >> 1], wq + (size_t)(st * 32 + 4 * e) * (HD * 2), lds0 + (st % 3) * 16384 + (wr * 4 + e) * 1024);
  };
  auto issue_x = [&](int j) {
#pragma unroll
    for (int e = 0; e < 6; ++e)
      dma16(voff[2 + (e & 1)], x + (size_t)(wr * 48 + 8 * e) * (DM * 2) + j * 128,
            lds0 + K1_XBASE + (j & 1) * 24576 + (wr * 6 + e) * 1024);
  };
  auto issue_kv = [&](int kind, int uu, int pc, int blkidx) {
    const char* src = (kind ? vc : kc) + ((size_t)(uu >> 1) * HQ + (uu & 1)) * (SQ * DH * 2) + pc * 8192;
#pragma unroll
    for (int e = 0; e < 2; ++e) dma16_nt(voff[2 * kind + e], src, lds0 + blkidx * 8192 + (wr * 2 + e) * 1024);
  };

  const int fl = qp | ((g & 1) << 2);
  unsigned trw[8];
#pragma unroll
  for (int dt = 0; dt < 8; ++dt) trw[dt] = lds0 + hh * 8192 + (8 * g + qp) * 256 + 8 * p + ((dt ^ fl) << 5);
  unsigned xrd[2];
#pragma unroll
  for (int hf = 0; hf < 2; ++hf)
    xrd[hf] = lds0 + K1_XBASE + (bb * SQ + qh * 48 + c) * 128 + (((4 * hf + g) ^ ((c >> 1) & 7)) << 4);

  f32x4 qacc[8][3];
  bf16x8 af[8], xf[3];
  auto read_a = [&](int st, int dt) {
    const unsigned a0 = trw[dt] + (st % 3) * 16384;
    return cat8(tr_read(a0), tr_read(a0 + 4 * 256));
  };
  auto read_x = [&](int st, int t) { return lds_frag(xrd[st & 1] + ((st >> 1) & 1) * 24576 + t * 16 * 128); };

  if (wx_role) {
    issue_w(0); issue_x(0); issue_w(1); issue_w(2); issue_x(1);
    wait_vm<14>();
  } else {
    const unsigned dump = lds0 + 19 * 8192 + wr * 2048;
    dma4((unsigned)(((idx >> 2) * 64 + wr * 16 + (lane >> 2)) * (HD * 2) + (lane & 3) * 128), wq, dump);
    dma4((unsigned)(((idx & 3) * 48 + wr * 12) * (DM * 2) + lane * 128), x, dump + 256);
    dma4((unsigned)(((idx & 3) * 48 + wr * 12 + 8) * (DM * 2) + (lane & 31) * 128), x, dump + 512);
    issue_kv(0, 0, 0, 12); issue_kv(0, 1, 0, 13);
  }
  raw_barrier();
#pragma unroll
  for (int t = 0; t < 3; ++t) xf[t] = read_x(0, t);
#pragma unroll
  for (int dt = 0; dt < 8; ++dt) af[dt] = read_a(0, dt);

  auto step = [&](auto ic) {
    constexpr int s = decltype(ic)::value;
    STAMP(1 + s);
    if (wx_role) {
      if constexpr (s < 15) wait_vm<wait_before_barrier(s)>();
    }
    raw_barrier();
    if (wx_role) {
      if constexpr (n_w_at(s) > 0) issue_w(s + 3);
      if constexpr (n_x_at(s) > 0) issue_x((s + 3) / 2);
    } else {
      if constexpr (s < 6) issue_kv(0, (s + 2) & 3, (s + 2) >> 2, 12 + s + 2);
      if constexpr (s == 13) { issue_kv(0, 0, 2, 2); issue_kv(0, 1, 2, 3); issue_kv(0, 2, 2, 6); issue_kv(0, 3, 2, 7); issue_kv(1, 0, 0, 8); }
      if constexpr (s == 14) { issue_kv(1, 1, 0, 4); issue_kv(1, 2, 0, 5); }
      if constexpr (s == 15) { issue_kv(1, 3, 0, 0); issue_kv(1, 0, 1, 1); issue_kv(1, 1, 1, 9); issue_kv(1, 2, 1, 10); issue_kv(1, 3, 1, 11); }
    }
    bf16x8 xn[3], an[8];
    if constexpr (s < 15) {
#pragma unroll
      for (int t = 0; t < 3; ++t) xn[t] = read_x(s + 1, t);
      an[0] = read_a(s + 1, 0);
      an[1] = read_a(s + 1, 1);
    }
    __builtin_amdgcn_sched_barrier(0);
#pragma unroll
    for (int dt = 0; dt < 8; ++dt) {
#pragma unroll
      for (int t = 0; t < 3; ++t) {
        if constexpr (s == 0) mfma_init(qacc[dt][t], af[dt], xf[t]);
        else mfma_acc(qacc[dt][t], af[dt], xf[t]);
      }
      if constexpr (s < 15) {
        if (dt + 2 < 8) an[dt + 2] = read_a(s + 1, dt + 2);
      }
      __builtin_amdgcn_sched_barrier(0);
    }
    if constexpr (s < 15) {
#pragma unroll
      for (int t = 0; t < 3; ++t) xf[t] = xn[t];
#pragma unroll
      for (int dt = 0; dt < 8; ++dt) af[dt] = an[dt];
    }
  };
  step(IC<0>{}); step(IC<1>{}); step(IC<2>{}); step(IC<3>{}); step(IC<4>{}); step(IC<5>{}); step(IC<6>{}); step(IC<7>{});
  step(IC<8>{}); step(IC<9>{}); step(IC<10>{}); step(IC<11>{}); step(IC<12>{}); step(IC<13>{}); step(IC<14>{}); step(IC<15>{});

  asm volatile("s_nop 7\n\ts_nop 7" ::: "memory");
  __builtin_amdgcn_sched_barrier(0);
  STAMP(17);
  bf16x8 qf[4][3];
#pragma unroll
  for (int s = 0; s < 4; ++s)
#pragma unroll
    for (int t = 0; t < 3; ++t) {
      u32x4 r;
#pragma unroll
      for (int e = 0; e < 2; ++e) {
        const unsigned a = pack2(qacc[2 * s][t][2 * e], qacc[2 * s][t][2 * e + 1]);
        const unsigned bq = pack2(qacc[2 * s + 1][t][2 * e], qacc[2 * s + 1][t][2 * e + 1]);
        auto r1 = __builtin_amdgcn_permlane32_swap(a, bq, false, false);
        auto r2 = __builtin_amdgcn_permlane16_swap(r1[0], r1[1], false, false);
        r[e] = r2[0];
        r[2 + e] = r2[1];
      }
      qf[s][t] = __builtin_bit_cast(bf16x8, r);
    }

  if (!wx_role) wait_vm<16>();
  raw_barrier();

  f32x4 sacc[6][3];
#pragma unroll
  for (int kt = 0; kt < 6; ++kt)
#pragma unroll
    for (int t = 0; t < 3; ++t) sacc[kt][t] = (f32x4){0.f, 0.f, 0.f, 0.f};
#pragma unroll
  for (int pc = 0; pc < 3; ++pc) {
    const int kblk = pc < 2 ? 12 + pc * 4 + u : ((0x7632 >> (4 * u)) & 15);
    const unsigned kb = lds0 + kblk * 8192 + c * 256;
#pragma unroll
    for (int k2 = 0; k2 < 2; ++k2) {
#pragma unroll
      for (int s = 0; s < 4; ++s) {
        const bf16x8 kf = lds_frag(kb + k2 * 4096 + (((4 * s + g) ^ c) << 4));
#pragma unroll
        for (int t = 0; t < 3; ++t) sacc[pc * 2 + k2][t] = MFMA16(kf, qf[s][t], sacc[pc * 2 + k2][t]);
      }
    }
  }
  STAMP(18);
  raw_barrier();
  if (!wx_role) {
    issue_kv(1, 0, 2, 12); issue_kv(1, 1, 2, 13); issue_kv(1, 2, 2, 14); issue_kv(1, 3, 2, 15);
    dma4((unsigned)(((xcd & 3) * 32 + idx) * 32768 + wr * 8192 + lane * 128), wo, lds0 + 19 * 8192 + wr * 2048);
  }

  const float cexp = 0.08838834764831845f * 1.4426950408889634f;
  bf16x8 pf[3][3];
  float rinv[3];
#pragma unroll
  for (int t = 0; t < 3; ++t) {
    float m = sacc[0][t][0];
#pragma unroll
    for (int kt = 0; kt < 6; ++kt)
#pragma unroll
      for (int r = 0; r < 4; ++r) m = fmaxf(m, sacc[kt][t][r]);
    m = xg_max(m);
    const float mc = m * cexp;
    float sum = 0.f;
#pragma unroll
    for (int kt = 0; kt < 6; ++kt)
#pragma unroll
      for (int r = 0; r < 4; ++r) {
        const float e = __builtin_amdgcn_exp2f(fmaf(sacc[kt][t][r], cexp, -mc));
        sacc[kt][t][r] = e;
        sum += e;
      }
    sum = xg_sum(sum);
    rinv[t] = 1.0f / sum;
#pragma unroll
    for (int s = 0; s < 3; ++s) {
      u32x4 r;
      r[0] = pack2(sacc[2 * s][t][0], sacc[2 * s][t][1]);
      r[1] = pack2(sacc[2 * s][t][2], sacc[2 * s][t][3]);
      r[2] = pack2(sacc[2 * s + 1][t][0], sacc[2 * s + 1][t][1]);
      r[3] = pack2(sacc[2 * s + 1][t][2], sacc[2 * s + 1][t][3]);
      pf[s][t] = __builtin_bit_cast(bf16x8, r);
    }
  }

  STAMP(19);
  f32x4 oacc[8][3];
#pragma unroll
  for (int dt = 0; dt < 8; ++dt)
#pragma unroll
    for (int t = 0; t < 3; ++t) oacc[dt][t] = (f32x4){0.f, 0.f, 0.f, 0.f};
  const unsigned vlane = lds0 + (4 * g + qp) * 256 + 8 * p;
#pragma unroll
  for (int s = 0; s < 3; ++s) {
    if (!wx_role) {
      if (s == 0) wait_vm<17>();
      if (s == 1) wait_vm<9>();
      if (s == 2) wait_vm<1>();
    }
    raw_barrier();
    const int vblk = s == 0 ? ((0x0548 >> (4 * u)) & 15) : s == 1 ? ((0xBA91 >> (4 * u)) & 15) : 12 + u;
    const unsigned vb = vlane + vblk * 8192;
#pragma unroll
    for (int dt = 0; dt < 8; ++dt) {
      const unsigned a0 = vb + ((dt ^ fl) << 5);
      const bf16x8 vf = cat8(tr_read(a0), tr_read(a0 + 16 * 256));
#pragma unroll
      for (int t = 0; t < 3; ++t) oacc[dt][t] = MFMA16(vf, pf[s][t], oacc[dt][t]);
    }
  }

  STAMP(20);
  u32x4* ow = o_ws + ((((size_t)(blk * NB + b) * HQ + h) * 4) * 6 + qh * 3) * 64 + lane;
#pragma unroll
  for (int s = 0; s < 4; ++s)
#pragma unroll
    for (int t = 0; t < 3; ++t) {
      const float ri = rinv[t];
      u32x4 r;
      r[0] = pack2(oacc[2 * s][t][0] * ri, oacc[2 * s][t][1] * ri);
      r[1] = pack2(oacc[2 * s][t][2] * ri, oacc[2 * s][t][3] * ri);
      r[2] = pack2(oacc[2 * s + 1][t][0] * ri, oacc[2 * s + 1][t][1] * ri);
      r[3] = pack2(oacc[2 * s + 1][t][2] * ri, oacc[2 * s + 1][t][3] * ri);
      ow[(s * 6 + t) * 64] = r;
    }
  STAMP(21);
}

constexpr int K2_LDS = 65536;

__global__ __launch_bounds__(512, 2) void oproj_kernel(const u32x4* __restrict__ o_ws,
                                                       const short* __restrict__ woA,
                                                       const short* __restrict__ woB,
                                                       unsigned short* __restrict__ out) {
  extern __shared__ __attribute__((aligned(16))) char smem[];
  const int tid = threadIdx.x;
  const int lane = tid & 63;
  const int wave = __builtin_amdgcn_readfirstlane(tid >> 6);
  const int c = lane & 15, g = lane >> 4, qp = (lane & 15) >> 2, p = lane & 3;
  const int bid = blockIdx.x;
  const int xcd = bid & 7, idx = bid >> 3;
  const int blk = xcd >> 2;
  const int b = (xcd & 3) * 4 + (idx >> 3);
  const int nt = idx & 7;
  const int n0 = nt * 64;
  const short* __restrict__ wo = blk ? woB : woA;

  const unsigned lds0 = (unsigned)(uintptr_t)smem;
  char* wbuf = smem + wave * 8192;
  const unsigned ldsW = lds0 + wave * 8192;

  const int srow = lane >> 3, sch = lane & 7;
  const unsigned st_off = srow * 128 + ((((sch >> 1) ^ ((srow >> 1) & 3)) << 5) | ((sch & 1) << 4));
  const short* wsrc = wo + (size_t)(wave * 512 + srow) * DM + n0 + sch * 8;
  const int fl = (2 * g + (qp >> 1)) & 3;
  unsigned wrd[4];
#pragma unroll
  for (int j = 0; j < 4; ++j) wrd[j] = ldsW + (4 * g + qp) * 128 + 8 * p + ((j ^ fl) << 5);

  const u32x4* osrc = o_ws + (((size_t)(blk * NB + b) * HQ + wave * 4) * 4) * 6 * 64 + lane;

  f32x4 acc[4][6];
#pragma unroll
  for (int j = 0; j < 4; ++j)
#pragma unroll
    for (int t = 0; t < 6; ++t) acc[j][t] = (f32x4){0.f, 0.f, 0.f, 0.f};

  u32x4 wst[4];
  u32x4 of[2][6];
#pragma unroll
  for (int i = 0; i < 4; ++i) wst[i] = *(const u32x4*)(wsrc + (size_t)(8 * i) * DM);
#pragma unroll
  for (int t = 0; t < 6; ++t) of[0][t] = osrc[t * 64];
#pragma unroll
  for (int i = 0; i < 4; ++i) *(u32x4*)(wbuf + st_off + i * 8 * 128) = wst[i];
#pragma unroll
  for (int i = 0; i < 4; ++i) wst[i] = *(const u32x4*)(wsrc + (size_t)(32 + 8 * i) * DM);

#pragma unroll 2
  for (int ks = 0; ks < 16; ++ks) {
    if (ks + 1 < 16) {
#pragma unroll
      for (int i = 0; i < 4; ++i) *(u32x4*)(wbuf + ((ks + 1) & 1) * 4096 + st_off + i * 8 * 128) = wst[i];
    }
    if (ks + 2 < 16) {
#pragma unroll
      for (int i = 0; i < 4; ++i) wst[i] = *(const u32x4*)(wsrc + (size_t)(32 * (ks + 2) + 8 * i) * DM);
    }
    if (ks + 1 < 16) {
#pragma unroll
      for (int t = 0; t < 6; ++t) of[(ks + 1) & 1][t] = osrc[((ks + 1) * 6 + t) * 64];
    }
    __builtin_amdgcn_sched_barrier(0);
#pragma unroll
    for (int j = 0; j < 4; ++j) {
      const unsigned a0 = wrd[j] + (ks & 1) * 4096;
      const bf16x8 af = cat8(tr_read(a0), tr_read(a0 + 16 * 128));
#pragma unroll
      for (int t = 0; t < 6; ++t) acc[j][t] = MFMA16(af, __builtin_bit_cast(bf16x8, of[ks & 1][t]), acc[j][t]);
    }
  }

  f32x4* red = (f32x4*)smem;
#pragma unroll
  for (int rd = 0; rd < 3; ++rd) {
    __syncthreads();
#pragma unroll
    for (int j = 0; j < 4; ++j)
#pragma unroll
      for (int tt = 0; tt < 2; ++tt) red[(wave * 8 + j * 2 + tt) * 64 + lane] = acc[j][rd * 2 + tt];
    __syncthreads();
    f32x4 s = red[tid];
#pragma unroll
    for (int w = 1; w < 8; ++w) s += red[w * 512 + tid];
    const int j = wave >> 1, tt = wave & 1;
    const int q = (rd * 2 + tt) * 16 + c;
    const int n = n0 + j * 16 + 4 * g;
    u32x2 r;
    r[0] = pack2(s[0], s[1]);
    r[1] = pack2(s[2], s[3]);
    *(u32x2*)(out + (size_t)(b * SQ + q) * 1024 + blk * 512 + n) = r;
  }
}

}

extern "C" void kernel_launch(void* const* d_in, const int* in_sizes, int n_in, void* d_out, int out_size,
                              void* d_ws, size_t ws_size, hipStream_t stream) {
  const short* xA = (const short*)d_in[0];
  const short* kA = (const short*)d_in[1];
  const short* vA = (const short*)d_in[2];
  const short* wqA = (const short*)d_in[3];
  const short* woA = (const short*)d_in[4];
  const short* xB = (const short*)d_in[5];
  const short* kB = (const short*)d_in[6];
  const short* vB = (const short*)d_in[7];
  const short* wqB = (const short*)d_in[8];
  const short* woB = (const short*)d_in[9];
  u32x4* o_ws = (u32x4*)d_ws;
  (void)hipFuncSetAttribute((const void*)qattn_kernel, hipFuncAttributeMaxDynamicSharedMemorySize, K1_LDS);
  (void)hipFuncSetAttribute((const void*)oproj_kernel, hipFuncAttributeMaxDynamicSharedMemorySize, K2_LDS);
  hipLaunchKernelGGL(qattn_kernel, dim3(256), dim3(512), K1_LDS, stream, xA, kA, vA, wqA, woA, xB, kB, vB, wqB,
                     woB, o_ws);
  hipLaunchKernelGGL(oproj_kernel, dim3(256), dim3(512), K2_LDS, stream, (const u32x4*)o_ws, woA, woB,
                     (unsigned short*)d_out);
}
```

```cpp
#include <hip/hip_runtime.h>
#include <hip/hip_bf16.h>
#include <stdint.h>

typedef __attribute__((ext_vector_type(8))) short bf16x8;
typedef __attribute__((ext_vector_type(4))) short s16x4;
typedef __attribute__((ext_vector_type(4))) float f32x4;
typedef __attribute__((ext_vector_type(2))) float f32x2;
typedef __attribute__((ext_vector_type(4))) unsigned u32x4;
typedef __attribute__((ext_vector_type(2))) unsigned u32x2;
typedef __attribute__((ext_vector_type(2))) __bf16 bf16x2_t;
typedef __attribute__((address_space(3))) s16x4* lds_s16x4_ptr;

#define DI __device__ __forceinline__
#define MFMA16(a, b, c) __builtin_amdgcn_mfma_f32_16x16x32_bf16((a), (b), (c), 0, 0, 0)

#define STAMP(i)
namespace {

constexpr int NB = 16, SQ = 96, DM = 512, HQ = 32, DH = 128, HD = HQ * DH;

DI unsigned pack2(float a, float b) {
  f32x2 v = {a, b};
  return __builtin_bit_cast(unsigned, __builtin_convertvector(v, bf16x2_t));
}
DI s16x4 tr_read(unsigned lds_addr) {
  return __builtin_amdgcn_ds_read_tr16_b64_v4i16((lds_s16x4_ptr)(uintptr_t)lds_addr);
}
DI bf16x8 cat8(s16x4 lo, s16x4 hi) { return __builtin_shufflevector(lo, hi, 0, 1, 2, 3, 4, 5, 6, 7); }
DI float xg_max(float x) {
  auto s = __builtin_amdgcn_permlane16_swap(__float_as_uint(x), __float_as_uint(x), false, false);
  x = fmaxf(__uint_as_float(s[0]), __uint_as_float(s[1]));
  auto t = __builtin_amdgcn_permlane32_swap(__float_as_uint(x), __float_as_uint(x), false, false);
  return fmaxf(__uint_as_float(t[0]), __uint_as_float(t[1]));
}
DI float xg_sum(float x) {
  auto s = __builtin_amdgcn_permlane16_swap(__float_as_uint(x), __float_as_uint(x), false, false);
  x = __uint_as_float(s[0]) + __uint_as_float(s[1]);
  auto t = __builtin_amdgcn_permlane32_swap(__float_as_uint(x), __float_as_uint(x), false, false);
  return __uint_as_float(t[0]) + __uint_as_float(t[1]);
}

constexpr int K1_LDS = 160 * 1024;
constexpr int K1_XBASE = 48 * 1024;
#define KV_NT 1

constexpr int n_w_at(int s) { return (s + 3 <= 15) ? 4 : 0; }
constexpr int n_x_at(int s) { return ((s & 1) && ((s + 3) / 2 <= 7)) ? 6 : 0; }
constexpr int issued_through(int s) {
  int n = 24;
  for (int j = 0; j <= s; ++j) n += n_w_at(j) + n_x_at(j);
  return n;
}
constexpr int pos_w(int st) {
  return st == 0 ? 4 : st == 1 ? 14 : st == 2 ? 18 : issued_through(st - 4) + 4;
}
constexpr int pos_x(int j) { return j == 0 ? 10 : j == 1 ? 24 : issued_through(2 * j - 3); }
constexpr int cmax(int a, int b) { return a > b ? a : b; }
constexpr int wait_before_barrier(int s) {
  return issued_through(s - 1) - cmax(pos_w(s + 1), pos_x((s + 1) >> 1));
}

template <int N> DI void wait_vm() { asm volatile("s_waitcnt vmcnt(%0)" ::"n"(N) : "memory"); }
DI void raw_barrier() {
  asm volatile("s_waitcnt lgkmcnt(0)" ::: "memory");
  __builtin_amdgcn_s_barrier();
  asm volatile("" ::: "memory");
}
DI void dma16(unsigned voff, const void* sbase, unsigned lds_dst) {
  unsigned keep;
  asm volatile("s_mov_b32 %0, m0\n\ts_mov_b32 m0, %3\n\ts_nop 0\n\tglobal_load_lds_dwordx4 %1, %2\n\ts_mov_b32 m0, %0"
               : "=&s"(keep) : "v"(voff), "s"(sbase), "s"(lds_dst) : "memory");
}
DI void dma16_nt(unsigned voff, const void* sbase, unsigned lds_dst) {
  unsigned keep;
  asm volatile("s_mov_b32 %0, m0\n\ts_mov_b32 m0, %3\n\ts_nop 0\n\tglobal_load_lds_dwordx4 %1, %2 nt\n\ts_mov_b32 m0, %0"
               : "=&s"(keep) : "v"(voff), "s"(sbase), "s"(lds_dst) : "memory");
}
DI void gload16_nt(u32x4& r, unsigned voff, const void* sbase) {
  asm volatile("global_load_dwordx4 %0, %1, %2 nt" : "=v"(r) : "v"(voff), "s"(sbase) : "memory");
}
DI void pin(u32x4& r) { asm volatile("" : "+v"(r)); }
typedef __attribute__((address_space(3))) u32x4* lds_u32x4_ptr;
DI void lds_store16(unsigned a, u32x4 v) { *(lds_u32x4_ptr)(uintptr_t)a = v; }
DI void mfma_acc(f32x4& c, bf16x8 a, bf16x8 b) {
  asm volatile("v_mfma_f32_16x16x32_bf16 %0, %1, %2, %0" : "+v"(c) : "v"(a), "v"(b));
}
DI void mfma_init(f32x4& c, bf16x8 a, bf16x8 b) {
  asm volatile("v_mfma_f32_16x16x32_bf16 %0, %1, %2, 0" : "=&v"(c) : "v"(a), "v"(b));
}
template <int V> struct IC { static constexpr int value = V; };
typedef const __attribute__((address_space(3))) bf16x8* lds_frag_ptr;
DI bf16x8 lds_frag(unsigned a) { return *(lds_frag_ptr)(uintptr_t)a; }

__global__ __launch_bounds__(512, 2) void qattn_kernel(
    const short* __restrict__ xA, const short* __restrict__ kA, const short* __restrict__ vA,
    const short* __restrict__ wqA, const short* __restrict__ woA, const short* __restrict__ xB,
    const short* __restrict__ kB, const short* __restrict__ vB, const short* __restrict__ wqB,
    const short* __restrict__ woB, u32x4* __restrict__ o_ws) {
  extern __shared__ __attribute__((aligned(16))) char smem[];
  const int tid = threadIdx.x;
  const int lane = tid & 63;
  const int wave = __builtin_amdgcn_readfirstlane(tid >> 6);
  const int c = lane & 15, g = lane >> 4, qp = (lane & 15) >> 2, p = lane & 3;
  const int bid = blockIdx.x;
  const int xcd = bid & 7, idx = bid >> 3;
  const int blk = xcd >> 2;
  const int h0 = ((xcd & 3) * 4 + (idx & 3)) * 2;
  const int b0 = (idx >> 2) * 2;
  const int u = wave >> 1, qh = wave & 1;
  const int bb = u >> 1, hh = u & 1;
  const int b = b0 + bb, h = h0 + hh;
  const bool wx_role = wave < 4;
  const int wr = wave & 3;

  const char* __restrict__ x = (const char*)(blk ? xB : xA) + (size_t)b0 * SQ * DM * 2;
  const char* __restrict__ kc = (const char*)(blk ? kB : kA) + ((size_t)b0 * HQ + h0) * (SQ * DH * 2);
  const char* __restrict__ vc = (const char*)(blk ? vB : vA) + ((size_t)b0 * HQ + h0) * (SQ * DH * 2);
  const char* __restrict__ wq = (const char*)(blk ? wqB : wqA) + h0 * DH * 2;
  const char* __restrict__ wo = (const char*)(blk ? woB : woA);

  const unsigned lds0 = (unsigned)(uintptr_t)smem;
  STAMP(0);

  unsigned voff[4];
  if (wx_role) {
#pragma unroll
    for (int e = 0; e < 2; ++e) {
      const int f = g | (e << 2);
      const int ch = (((c >> 1) ^ f) << 1) | (c & 1);
      voff[e] = (unsigned)((16 * (wr & 1) + g) * (HD * 2) + (wr >> 1) * (DH * 2) + ch * 16);
      const int chx = (lane & 7) ^ ((e << 2) | g);
      voff[2 + e] = (unsigned)((lane >> 3) * (DM * 2) + chx * 16);
    }
  } else {
#pragma unroll
    for (int e = 0; e < 2; ++e) {
      const int row = 4 * (2 * wr + e) + g;
      voff[e] = (unsigned)(row * 256 + ((c ^ (row & 15)) << 4));
      voff[2 + e] = (unsigned)(row * 256 + ((((((c >> 1) ^ (row & 7)) << 1) | (c & 1))) << 4));
    }
  }

  auto issue_w1 = [&](int st, int e) {
    dma16(voff[e >> 1], wq + (size_t)(st * 32 + 4 * e) * (HD * 2), lds0 + (st % 3) * 16384 + (wr * 4 + e) * 1024);
  };
  auto issue_x1 = [&](int j, int e) {
    dma16(voff[2 + (e & 1)], x + (size_t)(wr * 48 + 8 * e) * (DM * 2) + j * 128,
          lds0 + K1_XBASE + (j & 1) * 24576 + (wr * 6 + e) * 1024);
  };
  auto issue_w = [&](int st) {
#pragma unroll
    for (int e = 0; e < 4; ++e) issue_w1(st, e);
  };
  auto issue_x = [&](int j) {
#pragma unroll
    for (int e = 0; e < 6; ++e) issue_x1(j, e);
  };
  auto kv_src = [&](int kind, int uu, int pc) {
    return (kind ? vc : kc) + ((size_t)(uu >> 1) * HQ + (uu & 1)) * (SQ * DH * 2) + pc * 8192;
  };
  auto issue_kv1 = [&](int kind, int uu, int pc, int blkidx, int e) {
    dma16_nt(voff[2 * kind + e], kv_src(kind, uu, pc), lds0 + blkidx * 8192 + (wr * 2 + e) * 1024);
  };
  u32x4 kst[8], vst[8];
  auto issue_early = [&](int n) {
    const int pq = n >> 1, e = n & 1;
    if (pq < 8) issue_kv1(0, pq & 3, pq >> 2, 12 + pq, e);
    else if (pq < 12) gload16_nt(kst[n - 16], voff[e], kv_src(0, pq & 3, 2));
    else gload16_nt(vst[n - 24], voff[2 + e], kv_src(1, pq & 3, 2));
  };
  auto issue_late = [&](int n) {
    const int pq = n >> 1, e = n & 1;
    issue_kv1(1, pq & 3, pq >> 2, (0xBA910548u >> (4 * pq)) & 15, e);
  };

  const int fl = qp | ((g & 1) << 2);
  unsigned trw[8];
#pragma unroll
  for (int dt = 0; dt < 8; ++dt) trw[dt] = lds0 + hh * 8192 + (8 * g + qp) * 256 + 8 * p + ((dt ^ fl) << 5);
  unsigned xrd[2];
#pragma unroll
  for (int hf = 0; hf < 2; ++hf)
    xrd[hf] = lds0 + K1_XBASE + (bb * SQ + qh * 48 + c) * 128 + (((4 * hf + g) ^ ((c >> 1) & 7)) << 4);

  f32x4 qacc[8][3];
  bf16x8 af[8], xf[3];
  auto read_a = [&](int st, int dt) {
    const unsigned a0 = trw[dt] + (st % 3) * 16384;
    return cat8(tr_read(a0), tr_read(a0 + 4 * 256));
  };
  auto read_x = [&](int st, int t) { return lds_frag(xrd[st & 1] + ((st >> 1) & 1) * 24576 + t * 16 * 128); };

  if (wx_role) {
    issue_w(0); issue_x(0); issue_w(1);
    STAMP(1);
    wait_vm<4>();
  } else {
    STAMP(1);
  }
  raw_barrier();
  STAMP(2);
  if (wx_role) { issue_w(2); issue_x(1); }
#pragma unroll
  for (int t = 0; t < 3; ++t) xf[t] = read_x(0, t);
#pragma unroll
  for (int dt = 0; dt < 8; ++dt) af[dt] = read_a(0, dt);

  auto step = [&](auto ic) {
    constexpr int s = decltype(ic)::value;
    STAMP(3 + s);
    if (wx_role) {
      if constexpr (s < 15) wait_vm<wait_before_barrier(s)>();
    }
    raw_barrier();
    if constexpr (s == 13) {
      if (!wx_role) {
        wait_vm<8>();
#pragma unroll
        for (int n = 0; n < 8; ++n) {
          pin(kst[n]);
          lds_store16(lds0 + ((0x7632 >> (4 * (n >> 1))) & 15) * 8192 + (wr * 2 + (n & 1)) * 1024 + lane * 16, kst[n]);
        }
      }
    }
    bf16x8 xn[3], an[8];
    if constexpr (s < 15) {
#pragma unroll
      for (int t = 0; t < 3; ++t) xn[t] = read_x(s + 1, t);
      an[0] = read_a(s + 1, 0);
      an[1] = read_a(s + 1, 1);
    }
    __builtin_amdgcn_sched_barrier(0);
    auto group = [&](auto id) {
      constexpr int dt = decltype(id)::value;
#pragma unroll
      for (int t = 0; t < 3; ++t) {
        if constexpr (s == 0) mfma_init(qacc[dt][t], af[dt], xf[t]);
        else mfma_acc(qacc[dt][t], af[dt], xf[t]);
      }
      if constexpr (s < 15 && dt + 2 < 8) an[dt + 2] = read_a(s + 1, dt + 2);
      constexpr bool has_w = n_w_at(s) > 0 && dt < 4;
      constexpr bool has_x = n_x_at(s) > 0 && dt >= 4;
      constexpr int ne0 = s < 6 ? 3 * s : 18 + 2 * (s - 6);
      constexpr int nec = s < 6 ? 3 : s < 13 ? 2 : 0;
      constexpr bool has_e = (dt & 1) == 0 && (dt >> 1) < nec;
      constexpr int nl0 = s == 13 ? 0 : s == 14 ? 2 : 6;
      constexpr int nlc = s == 13 ? 2 : s == 14 ? 4 : s == 15 ? 10 : 0;
      constexpr bool has_l = dt < nlc;
      if constexpr (has_w || has_x || has_e || has_l) {
        if (wx_role) {
          if constexpr (has_w) issue_w1(s + 3, dt);
          if constexpr (has_x) {
            if constexpr (dt < 6) { issue_x1((s + 3) / 2, 2 * (dt - 4)); issue_x1((s + 3) / 2, 2 * (dt - 4) + 1); }
            else issue_x1((s + 3) / 2, dt - 2);
          }
        } else {
          if constexpr (has_e) issue_early(ne0 + (dt >> 1));
          if constexpr (has_l) {
            issue_late(nl0 + dt);
            if constexpr (nlc > 8 && dt >= 6) issue_late(nl0 + dt + 2);
          }
        }
      }
      __builtin_amdgcn_sched_barrier(0);
    };
    group(IC<0>{}); group(IC<1>{}); group(IC<2>{}); group(IC<3>{}); group(IC<4>{}); group(IC<5>{}); group(IC<6>{}); group(IC<7>{});
    if constexpr (s < 15) {
#pragma unroll
      for (int t = 0; t < 3; ++t) xf[t] = xn[t];
#pragma unroll
      for (int dt = 0; dt < 8; ++dt) af[dt] = an[dt];
    }
  };
  step(IC<0>{}); step(IC<1>{}); step(IC<2>{}); step(IC<3>{}); step(IC<4>{}); step(IC<5>{}); step(IC<6>{}); step(IC<7>{});
  step(IC<8>{}); step(IC<9>{}); step(IC<10>{}); step(IC<11>{}); step(IC<12>{}); step(IC<13>{}); step(IC<14>{}); step(IC<15>{});

  asm volatile("s_nop 7\n\ts_nop 7" ::: "memory");
  __builtin_amdgcn_sched_barrier(0);
  int lane2 = lane;
  asm volatile("" : "+v"(lane2));
  const int c2 = lane2 & 15, g2 = lane2 >> 4, qp2 = (lane2 & 15) >> 2, p2 = lane2 & 3;
  const int fl2 = qp2 | ((g2 & 1) << 2);
  STAMP(19);
  bf16x8 qf[4][3];
#pragma unroll
  for (int s = 0; s < 4; ++s)
#pragma unroll
    for (int t = 0; t < 3; ++t) {
      u32x4 r;
#pragma unroll
      for (int e = 0; e < 2; ++e) {
        const unsigned a = pack2(qacc[2 * s][t][2 * e], qacc[2 * s][t][2 * e + 1]);
        const unsigned bq = pack2(qacc[2 * s + 1][t][2 * e], qacc[2 * s + 1][t][2 * e + 1]);
        auto r1 = __builtin_amdgcn_permlane32_swap(a, bq, false, false);
        auto r2 = __builtin_amdgcn_permlane16_swap(r1[0], r1[1], false, false);
        r[e] = r2[0];
        r[2 + e] = r2[1];
      }
      qf[s][t] = __builtin_bit_cast(bf16x8, r);
    }

  STAMP(20);
  STAMP(21);

  f32x4 sacc[6][3];
#pragma unroll
  for (int kt = 0; kt < 6; ++kt)
#pragma unroll
    for (int t = 0; t < 3; ++t) sacc[kt][t] = (f32x4){0.f, 0.f, 0.f, 0.f};
#pragma unroll
  for (int pc = 0; pc < 3; ++pc) {
    const int kblk = pc < 2 ? 12 + pc * 4 + u : ((0x7632 >> (4 * u)) & 15);
    const unsigned kb = lds0 + kblk * 8192 + c2 * 256;
#pragma unroll
    for (int k2 = 0; k2 < 2; ++k2) {
#pragma unroll
      for (int s = 0; s < 4; ++s) {
        const bf16x8 kf = lds_frag(kb + k2 * 4096 + (((4 * s + g2) ^ c2) << 4));
#pragma unroll
        for (int t = 0; t < 3; ++t) sacc[pc * 2 + k2][t] = MFMA16(kf, qf[s][t], sacc[pc * 2 + k2][t]);
      }
    }
  }
  STAMP(22);
  raw_barrier();
  if (!wx_role) {
    wait_vm<16>();
#pragma unroll
    for (int n = 0; n < 8; ++n) {
      pin(vst[n]);
      lds_store16(lds0 + (12 + (n >> 1)) * 8192 + (wr * 2 + (n & 1)) * 1024 + lane2 * 16, vst[n]);
    }
  }
  STAMP(23);

  const float cexp = 0.08838834764831845f * 1.4426950408889634f;
  bf16x8 pf[3][3];
  float rinv[3];
#pragma unroll
  for (int t = 0; t < 3; ++t) {
    float m = sacc[0][t][0];
#pragma unroll
    for (int kt = 0; kt < 6; ++kt)
#pragma unroll
      for (int r = 0; r < 4; ++r) m = fmaxf(m, sacc[kt][t][r]);
    m = xg_max(m);
    const float mc = m * cexp;
    float sum = 0.f;
#pragma unroll
    for (int kt = 0; kt < 6; ++kt)
#pragma unroll
      for (int r = 0; r < 4; ++r) {
        const float e = __builtin_amdgcn_exp2f(fmaf(sacc[kt][t][r], cexp, -mc));
        sacc[kt][t][r] = e;
        sum += e;
      }
    sum = xg_sum(sum);
    rinv[t] = 1.0f / sum;
#pragma unroll
    for (int s = 0; s < 3; ++s) {
      u32x4 r;
      r[0] = pack2(sacc[2 * s][t][0], sacc[2 * s][t][1]);
      r[1] = pack2(sacc[2 * s][t][2], sacc[2 * s][t][3]);
      r[2] = pack2(sacc[2 * s + 1][t][0], sacc[2 * s + 1][t][1]);
      r[3] = pack2(sacc[2 * s + 1][t][2], sacc[2 * s + 1][t][3]);
      pf[s][t] = __builtin_bit_cast(bf16x8, r);
    }
  }

  STAMP(24);
  f32x4 oacc[8][3];
#pragma unroll
  for (int dt = 0; dt < 8; ++dt)
#pragma unroll
    for (int t = 0; t < 3; ++t) oacc[dt][t] = (f32x4){0.f, 0.f, 0.f, 0.f};
  const unsigned vlane = lds0 + (4 * g2 + qp2) * 256 + 8 * p2;
#pragma unroll
  for (int si = 0; si < 3; ++si) {
    const int s = si == 0 ? 2 : si - 1;
    if (!wx_role) {
      if (si == 1) wait_vm<8>();
      if (si == 2) wait_vm<0>();
    }
    raw_barrier();
    STAMP(25 + si);
    const int vblk = s == 0 ? ((0x0548 >> (4 * u)) & 15) : s == 1 ? ((0xBA91 >> (4 * u)) & 15) : 12 + u;
    const unsigned vb = vlane + vblk * 8192;
#pragma unroll
    for (int dt = 0; dt < 8; ++dt) {
      const unsigned a0 = vb + ((dt ^ fl2) << 5);
      const bf16x8 vf = cat8(tr_read(a0), tr_read(a0 + 16 * 256));
#pragma unroll
      for (int t = 0; t < 3; ++t) oacc[dt][t] = MFMA16(vf, pf[s][t], oacc[dt][t]);
    }
  }

  STAMP(28);
  u32x4* ow = o_ws + ((((size_t)(blk * NB + b) * HQ + h) * 4) * 6 + qh * 3) * 64 + lane2;
#pragma unroll
  for (int s = 0; s < 4; ++s)
#pragma unroll
    for (int t = 0; t < 3; ++t) {
      const float ri = rinv[t];
      u32x4 r;
      r[0] = pack2(oacc[2 * s][t][0] * ri, oacc[2 * s][t][1] * ri);
      r[1] = pack2(oacc[2 * s][t][2] * ri, oacc[2 * s][t][3] * ri);
      r[2] = pack2(oacc[2 * s + 1][t][0] * ri, oacc[2 * s + 1][t][1] * ri);
      r[3] = pack2(oacc[2 * s + 1][t][2] * ri, oacc[2 * s + 1][t][3] * ri);
      ow[(s * 6 + t) * 64] = r;
    }
  STAMP(29);
}

constexpr int K2_LDS = 65536;

__global__ __launch_bounds__(512, 2) void oproj_kernel(const u32x4* __restrict__ o_ws,
                                                       const short* __restrict__ woA,
                                                       const short* __restrict__ woB,
                                                       unsigned short* __restrict__ out) {
  extern __shared__ __attribute__((aligned(16))) char smem[];
  const int tid = threadIdx.x;
  const int lane = tid & 63;
  const int wave = __builtin_amdgcn_readfirstlane(tid >> 6);
  const int c = lane & 15, g = lane >> 4, qp = (lane & 15) >> 2, p = lane & 3;
  const int bid = blockIdx.x;
  const int xcd = bid & 7, idx = bid >> 3;
  const int blk = xcd >> 2;
  const int b = (xcd & 3) * 4 + (idx >> 3);
  const int nt = idx & 7;
  const int n0 = nt * 64;
  const short* __restrict__ wo = blk ? woB : woA;

  const unsigned lds0 = (unsigned)(uintptr_t)smem;
  char* wbuf = smem + wave * 8192;
  const unsigned ldsW = lds0 + wave * 8192;

  const int srow = lane >> 3, sch = lane & 7;
  const unsigned st_off = srow * 128 + ((((sch >> 1) ^ ((srow >> 1) & 3)) << 5) | ((sch & 1) << 4));
  const short* wsrc = wo + (size_t)(wave * 512 + srow) * DM + n0 + sch * 8;
  const int fl = (2 * g + (qp >> 1)) & 3;
  unsigned wrd[4];
#pragma unroll
  for (int j = 0; j < 4; ++j) wrd[j] = ldsW + (4 * g + qp) * 128 + 8 * p + ((j ^ fl) << 5);

  const u32x4* osrc = o_ws + (((size_t)(blk * NB + b) * HQ + wave * 4) * 4) * 6 * 64 + lane;

  f32x4 acc[4][6];
#pragma unroll
  for (int j = 0; j < 4; ++j)
#pragma unroll
    for (int t = 0; t < 6; ++t) acc[j][t] = (f32x4){0.f, 0.f, 0.f, 0.f};

  u32x4 wst[4];
  u32x4 of[2][6];
#pragma unroll
  for (int i = 0; i < 4; ++i) wst[i] = *(const u32x4*)(wsrc + (size_t)(8 * i) * DM);
#pragma unroll
  for (int t = 0; t < 6; ++t) of[0][t] = osrc[t * 64];
#pragma unroll
  for (int i = 0; i < 4; ++i) *(u32x4*)(wbuf + st_off + i * 8 * 128) = wst[i];
#pragma unroll
  for (int i = 0; i < 4; ++i) wst[i] = *(const u32x4*)(wsrc + (size_t)(32 + 8 * i) * DM);

#pragma unroll 2
  for (int ks = 0; ks < 16; ++ks) {
    if (ks + 1 < 16) {
#pragma unroll
      for (int i = 0; i < 4; ++i) *(u32x4*)(wbuf + ((ks + 1) & 1) * 4096 + st_off + i * 8 * 128) = wst[i];
    }
    if (ks + 2 < 16) {
#pragma unroll
      for (int i = 0; i < 4; ++i) wst[i] = *(const u32x4*)(wsrc + (size_t)(32 * (ks + 2) + 8 * i) * DM);
    }
    if (ks + 1 < 16) {
#pragma unroll
      for (int t = 0; t < 6; ++t) of[(ks + 1) & 1][t] = osrc[((ks + 1) * 6 + t) * 64];
    }
    __builtin_amdgcn_sched_barrier(0);
#pragma unroll
    for (int j = 0; j < 4; ++j) {
      const unsigned a0 = wrd[j] + (ks & 1) * 4096;
      const bf16x8 af = cat8(tr_read(a0), tr_read(a0 + 16 * 128));
#pragma unroll
      for (int t = 0; t < 6; ++t) acc[j][t] = MFMA16(af, __builtin_bit_cast(bf16x8, of[ks & 1][t]), acc[j][t]);
    }
  }

  f32x4* red = (f32x4*)smem;
#pragma unroll
  for (int rd = 0; rd < 3; ++rd) {
    __syncthreads();
#pragma unroll
    for (int j = 0; j < 4; ++j)
#pragma unroll
      for (int tt = 0; tt < 2; ++tt) red[(wave * 8 + j * 2 + tt) * 64 + lane] = acc[j][rd * 2 + tt];
    __syncthreads();
    f32x4 s = red[tid];
#pragma unroll
    for (int w = 1; w < 8; ++w) s += red[w * 512 + tid];
    const int j = wave >> 1, tt = wave & 1;
    const int q = (rd * 2 + tt) * 16 + c;
    const int n = n0 + j * 16 + 4 * g;
    u32x2 r;
    r[0] = pack2(s[0], s[1]);
    r[1] = pack2(s[2], s[3]);
    *(u32x2*)(out + (size_t)(b * SQ + q) * 1024 + blk * 512 + n) = r;
  }
}

}

extern "C" void kernel_launch(void* const* d_in, const int* in_sizes, int n_in, void* d_out, int out_size,
                              void* d_ws, size_t ws_size, hipStream_t stream) {
  const short* xA = (const short*)d_in[0];
  const short* kA = (const short*)d_in[1];
  const short* vA = (const short*)d_in[2];
  const short* wqA = (const short*)d_in[3];
  const short* woA = (const short*)d_in[4];
  const short* xB = (const short*)d_in[5];
  const short* kB = (const short*)d_in[6];
  const short* vB = (const short*)d_in[7];
  const short* wqB = (const short*)d_in[8];
  const short* woB = (const short*)d_in[9];
  u32x4* o_ws = (u32x4*)d_ws;
  (void)hipFuncSetAttribute((const void*)qattn_kernel, hipFuncAttributeMaxDynamicSharedMemorySize, K1_LDS);
  (void)hipFuncSetAttribute((const void*)oproj_kernel, hipFuncAttributeMaxDynamicSharedMemorySize, K2_LDS);
  hipLaunchKernelGGL(qattn_kernel, dim3(256), dim3(512), K1_LDS, stream, xA, kA, vA, wqA, woA, xB, kB, vB, wqB,
                     woB, o_ws);
  hipLaunchKernelGGL(oproj_kernel, dim3(256), dim3(512), K2_LDS, stream, (const u32x4*)o_ws, woA, woB,
                     (unsigned short*)d_out);
}
```

```cpp
#include <hip/hip_runtime.h>
#include <hip/hip_bf16.h>
#include <stdint.h>

typedef __attribute__((ext_vector_type(8))) short bf16x8;
typedef __attribute__((ext_vector_type(4))) short s16x4;
typedef __attribute__((ext_vector_type(4))) float f32x4;
typedef __attribute__((ext_vector_type(2))) float f32x2;
typedef __attribute__((ext_vector_type(4))) unsigned u32x4;
typedef __attribute__((ext_vector_type(2))) unsigned u32x2;
typedef __attribute__((ext_vector_type(2))) __bf16 bf16x2_t;
typedef __attribute__((address_space(3))) s16x4* lds_s16x4_ptr;

#define DI __device__ __forceinline__
#define MFMA16(a, b, c) __builtin_amdgcn_mfma_f32_16x16x32_bf16((a), (b), (c), 0, 0, 0)

#define STAMP(i)
namespace {

constexpr int NB = 16, SQ = 96, DM = 512, HQ = 32, DH = 128, HD = HQ * DH;

DI unsigned pack2(float a, float b) {
  f32x2 v = {a, b};
  return __builtin_bit_cast(unsigned, __builtin_convertvector(v, bf16x2_t));
}
DI s16x4 tr_read(unsigned lds_addr) {
  return __builtin_amdgcn_ds_read_tr16_b64_v4i16((lds_s16x4_ptr)(uintptr_t)lds_addr);
}
DI bf16x8 cat8(s16x4 lo, s16x4 hi) { return __builtin_shufflevector(lo, hi, 0, 1, 2, 3, 4, 5, 6, 7); }
DI float xg_max(float x) {
  auto s = __builtin_amdgcn_permlane16_swap(__float_as_uint(x), __float_as_uint(x), false, false);
  x = fmaxf(__uint_as_float(s[0]), __uint_as_float(s[1]));
  auto t = __builtin_amdgcn_permlane32_swap(__float_as_uint(x), __float_as_uint(x), false, false);
  return fmaxf(__uint_as_float(t[0]), __uint_as_float(t[1]));
}
DI float xg_sum(float x) {
  auto s = __builtin_amdgcn_permlane16_swap(__float_as_uint(x), __float_as_uint(x), false, false);
  x = __uint_as_float(s[0]) + __uint_as_float(s[1]);
  auto t = __builtin_amdgcn_permlane32_swap(__float_as_uint(x), __float_as_uint(x), false, false);
  return __uint_as_float(t[0]) + __uint_as_float(t[1]);
}

constexpr int K1_LDS = 160 * 1024;
constexpr int K1_XBASE = 48 * 1024;
#define KV_NT 1

constexpr int n_w_at(int s) { return (s + 3 <= 15) ? 4 : 0; }
constexpr int n_x_at(int s) { return ((s & 1) && ((s + 3) / 2 <= 7)) ? 6 : 0; }
constexpr int issued_through(int s) {
  int n = 24;
  for (int j = 0; j <= s; ++j) n += n_w_at(j) + n_x_at(j);
  return n;
}
constexpr int pos_w(int st) {
  return st == 0 ? 4 : st == 1 ? 14 : st == 2 ? 18 : issued_through(st - 4) + 4;
}
constexpr int pos_x(int j) { return j == 0 ? 10 : j == 1 ? 24 : issued_through(2 * j - 3); }
constexpr int cmax(int a, int b) { return a > b ? a : b; }
constexpr int wait_before_barrier(int s) {
  return issued_through(s - 1) - cmax(pos_w(s + 1), pos_x((s + 1) >> 1));
}

template <int N> DI void wait_vm() { asm volatile("s_waitcnt vmcnt(%0)" ::"n"(N) : "memory"); }
DI void raw_barrier() {
  asm volatile("s_waitcnt lgkmcnt(0)" ::: "memory");
  __builtin_amdgcn_s_barrier();
  asm volatile("" ::: "memory");
}
DI void dma16(unsigned voff, const void* sbase, unsigned lds_dst) {
  unsigned keep;
  asm volatile("s_mov_b32 %0, m0\n\ts_mov_b32 m0, %3\n\ts_nop 0\n\tglobal_load_lds_dwordx4 %1, %2\n\ts_mov_b32 m0, %0"
               : "=&s"(keep) : "v"(voff), "s"(sbase), "s"(lds_dst) : "memory");
}
DI void dma16_nt(unsigned voff, const void* sbase, unsigned lds_dst) {
  unsigned keep;
  asm volatile("s_mov_b32 %0, m0\n\ts_mov_b32 m0, %3\n\ts_nop 0\n\tglobal_load_lds_dwordx4 %1, %2 nt\n\ts_mov_b32 m0, %0"
               : "=&s"(keep) : "v"(voff), "s"(sbase), "s"(lds_dst) : "memory");
}
DI void dma4(unsigned voff, const void* sbase, unsigned lds_dst) {
  unsigned keep;
  asm volatile("s_mov_b32 %0, m0\n\ts_mov_b32 m0, %3\n\ts_nop 0\n\tglobal_load_lds_dword %1, %2\n\ts_mov_b32 m0, %0"
               : "=&s"(keep) : "v"(voff), "s"(sbase), "s"(lds_dst) : "memory");
}
DI void gload16_nt(u32x4& r, unsigned voff, const void* sbase) {
  asm volatile("global_load_dwordx4 %0, %1, %2 nt" : "=v"(r) : "v"(voff), "s"(sbase) : "memory");
}
DI void pin(u32x4& r) { asm volatile("" : "+v"(r)); }
typedef __attribute__((address_space(3))) u32x4* lds_u32x4_ptr;
DI void lds_store16(unsigned a, u32x4 v) { *(lds_u32x4_ptr)(uintptr_t)a = v; }
DI void mfma_acc(f32x4& c, bf16x8 a, bf16x8 b) {
  asm volatile("v_mfma_f32_16x16x32_bf16 %0, %1, %2, %0" : "+v"(c) : "v"(a), "v"(b));
}
DI void mfma_init(f32x4& c, bf16x8 a, bf16x8 b) {
  asm volatile("v_mfma_f32_16x16x32_bf16 %0, %1, %2, 0" : "=&v"(c) : "v"(a), "v"(b));
}
template <int V> struct IC { static constexpr int value = V; };
typedef const __attribute__((address_space(3))) bf16x8* lds_frag_ptr;
DI bf16x8 lds_frag(unsigned a) { return *(lds_frag_ptr)(uintptr_t)a; }

__global__ __launch_bounds__(512, 2) void qattn_kernel(
    const short* __restrict__ xA, const short* __restrict__ kA, const short* __restrict__ vA,
    const short* __restrict__ wqA, const short* __restrict__ woA, const short* __restrict__ xB,
    const short* __restrict__ kB, const short* __restrict__ vB, const short* __restrict__ wqB,
    const short* __restrict__ woB, u32x4* __restrict__ o_ws) {
  extern __shared__ __attribute__((aligned(16))) char smem[];
  const int tid = threadIdx.x;
  const int lane = tid & 63;
  const int wave = __builtin_amdgcn_readfirstlane(tid >> 6);
  const int c = lane & 15, g = lane >> 4, qp = (lane & 15) >> 2, p = lane & 3;
  const int bid = blockIdx.x;
  const int xcd = bid & 7, idx = bid >> 3;
  const int blk = xcd >> 2;
  const int h0 = ((xcd & 3) * 4 + (idx & 3)) * 2;
  const int b0 = (idx >> 2) * 2;
  const int u = wave >> 1, qh = wave & 1;
  const int bb = u >> 1, hh = u & 1;
  const int b = b0 + bb, h = h0 + hh;
  const bool wx_role = wave < 4;
  const int wr = wave & 3;

  const char* __restrict__ x = (const char*)(blk ? xB : xA) + (size_t)b0 * SQ * DM * 2;
  const char* __restrict__ kc = (const char*)(blk ? kB : kA) + ((size_t)b0 * HQ + h0) * (SQ * DH * 2);
  const char* __restrict__ vc = (const char*)(blk ? vB : vA) + ((size_t)b0 * HQ + h0) * (SQ * DH * 2);
  const char* __restrict__ wq = (const char*)(blk ? wqB : wqA) + h0 * DH * 2;
  const char* __restrict__ wo = (const char*)(blk ? woB : woA);

  const unsigned lds0 = (unsigned)(uintptr_t)smem;
  STAMP(0);

  unsigned voff[4];
  if (wx_role) {
#pragma unroll
    for (int e = 0; e < 2; ++e) {
      const int f = g | (e << 2);
      const int ch = (((c >> 1) ^ f) << 1) | (c & 1);
      voff[e] = (unsigned)((16 * (wr & 1) + g) * (HD * 2) + (wr >> 1) * (DH * 2) + ch * 16);
      const int chx = (lane & 7) ^ ((e << 2) | g);
      voff[2 + e] = (unsigned)((lane >> 3) * (DM * 2) + chx * 16);
    }
  } else {
#pragma unroll
    for (int e = 0; e < 2; ++e) {
      const int row = 4 * (2 * wr + e) + g;
      voff[e] = (unsigned)(row * 256 + ((c ^ (row & 15)) << 4));
      voff[2 + e] = (unsigned)(row * 256 + ((((((c >> 1) ^ (row & 7)) << 1) | (c & 1))) << 4));
    }
  }

  const int rot = ((idx >> 2) + 2 * (idx & 3)) & 7;
  auto issue_w1 = [&](int st, int e) {
    dma16(voff[e >> 1], wq + (size_t)(((st + 2 * rot) & 15) * 32 + 4 * e) * (HD * 2),
          lds0 + (st % 3) * 16384 + (wr * 4 + e) * 1024);
  };
  auto issue_x1 = [&](int j, int e) {
    dma16(voff[2 + (e & 1)], x + (size_t)(wr * 48 + 8 * e) * (DM * 2) + ((j + rot) & 7) * 128,
          lds0 + K1_XBASE + (j & 1) * 24576 + (wr * 6 + e) * 1024);
  };
  auto issue_w = [&](int st) {
#pragma unroll
    for (int e = 0; e < 4; ++e) issue_w1(st, e);
  };
  auto issue_x = [&](int j) {
#pragma unroll
    for (int e = 0; e < 6; ++e) issue_x1(j, e);
  };
  auto kv_src = [&](int kind, int uu, int pc) {
    return (kind ? vc : kc) + ((size_t)(uu >> 1) * HQ + (uu & 1)) * (SQ * DH * 2) + pc * 8192;
  };
  auto issue_kv1 = [&](int kind, int uu, int pc, int blkidx, int e) {
    dma16_nt(voff[2 * kind + e], kv_src(kind, uu, pc), lds0 + blkidx * 8192 + (wr * 2 + e) * 1024);
  };
  u32x4 kst[8], vst[8];
  auto issue_early = [&](int n) {
    const int pq = n >> 1, e = n & 1;
    if (pq < 8) issue_kv1(0, pq & 3, pq >> 2, 12 + pq, e);
    else if (pq < 12) gload16_nt(kst[n - 16], voff[e], kv_src(0, pq & 3, 2));
    else gload16_nt(vst[n - 24], voff[2 + e], kv_src(1, pq & 3, 2));
  };
  auto issue_late = [&](int n) {
    const int pq = n >> 1, e = n & 1;
    issue_kv1(1, pq & 3, pq >> 2, (0xBA910548u >> (4 * pq)) & 15, e);
  };

  const int fl = qp | ((g & 1) << 2);
  unsigned trw[8];
#pragma unroll
  for (int dt = 0; dt < 8; ++dt) trw[dt] = lds0 + hh * 8192 + (8 * g + qp) * 256 + 8 * p + ((dt ^ fl) << 5);
  unsigned xrd[2];
#pragma unroll
  for (int hf = 0; hf < 2; ++hf)
    xrd[hf] = lds0 + K1_XBASE + (bb * SQ + qh * 48 + c) * 128 + (((4 * hf + g) ^ ((c >> 1) & 7)) << 4);

  f32x4 qacc[8][3];
  bf16x8 af[8], xf[3];
  auto read_a = [&](int st, int dt) {
    const unsigned a0 = trw[dt] + (st % 3) * 16384;
    return cat8(tr_read(a0), tr_read(a0 + 4 * 256));
  };
  auto read_x = [&](int st, int t) { return lds_frag(xrd[st & 1] + ((st >> 1) & 1) * 24576 + t * 16 * 128); };

  if (wx_role) {
    issue_w(0); issue_x(0); issue_w(1);
    STAMP(1);
    wait_vm<4>();
  } else {
    STAMP(1);
  }
  raw_barrier();
  STAMP(2);
  if (wx_role) { issue_w(2); issue_x(1); }
#pragma unroll
  for (int t = 0; t < 3; ++t) xf[t] = read_x(0, t);
#pragma unroll
  for (int dt = 0; dt < 8; ++dt) af[dt] = read_a(0, dt);

  auto step = [&](auto ic) {
    constexpr int s = decltype(ic)::value;
    STAMP(3 + s);
    if (wx_role) {
      if constexpr (s < 15) wait_vm<wait_before_barrier(s)>();
    }
    raw_barrier();
    if constexpr (s == 13) {
      if (!wx_role) {
        wait_vm<8>();
#pragma unroll
        for (int n = 0; n < 8; ++n) {
          pin(kst[n]);
          lds_store16(lds0 + ((0x7632 >> (4 * (n >> 1))) & 15) * 8192 + (wr * 2 + (n & 1)) * 1024 + lane * 16, kst[n]);
        }
      }
    }
    bf16x8 xn[3], an[8];
    if constexpr (s < 15) {
#pragma unroll
      for (int t = 0; t < 3; ++t) xn[t] = read_x(s + 1, t);
      an[0] = read_a(s + 1, 0);
      an[1] = read_a(s + 1, 1);
    }
    __builtin_amdgcn_sched_barrier(0);
    auto group = [&](auto id) {
      constexpr int dt = decltype(id)::value;
#pragma unroll
      for (int t = 0; t < 3; ++t) {
        if constexpr (s == 0) mfma_init(qacc[dt][t], af[dt], xf[t]);
        else mfma_acc(qacc[dt][t], af[dt], xf[t]);
      }
      if constexpr (s < 15 && dt + 2 < 8) an[dt + 2] = read_a(s + 1, dt + 2);
      constexpr bool has_w = n_w_at(s) > 0 && dt < 4;
      constexpr bool has_x = n_x_at(s) > 0 && dt >= 4;
      constexpr int ne0 = s < 6 ? 3 * s : 18 + 2 * (s - 6);
      constexpr int nec = s < 6 ? 3 : s < 13 ? 2 : 0;
      constexpr bool has_e = (dt & 1) == 0 && (dt >> 1) < nec;
      constexpr int nl0 = s == 13 ? 0 : s == 14 ? 2 : 6;
      constexpr int nlc = s == 13 ? 2 : s == 14 ? 4 : s == 15 ? 10 : 0;
      constexpr bool has_l = dt < nlc;
      if constexpr (has_w || has_x || has_e || has_l) {
        if (wx_role) {
          if constexpr (has_w) issue_w1(s + 3, dt);
          if constexpr (has_x) {
            if constexpr (dt < 6) { issue_x1((s + 3) / 2, 2 * (dt - 4)); issue_x1((s + 3) / 2, 2 * (dt - 4) + 1); }
            else issue_x1((s + 3) / 2, dt - 2);
          }
        } else {
          if constexpr (has_e) issue_early(ne0 + (dt >> 1));
          if constexpr (has_l) {
            issue_late(nl0 + dt);
            if constexpr (nlc > 8 && dt >= 6) issue_late(nl0 + dt + 2);
          }
        }
      }
      __builtin_amdgcn_sched_barrier(0);
    };
    group(IC<0>{}); group(IC<1>{}); group(IC<2>{}); group(IC<3>{}); group(IC<4>{}); group(IC<5>{}); group(IC<6>{}); group(IC<7>{});
    if constexpr (s < 15) {
#pragma unroll
      for (int t = 0; t < 3; ++t) xf[t] = xn[t];
#pragma unroll
      for (int dt = 0; dt < 8; ++dt) af[dt] = an[dt];
    }
  };
  step(IC<0>{}); step(IC<1>{}); step(IC<2>{}); step(IC<3>{}); step(IC<4>{}); step(IC<5>{}); step(IC<6>{}); step(IC<7>{});
  step(IC<8>{}); step(IC<9>{}); step(IC<10>{}); step(IC<11>{}); step(IC<12>{}); step(IC<13>{}); step(IC<14>{}); step(IC<15>{});

  asm volatile("s_nop 7\n\ts_nop 7" ::: "memory");
  __builtin_amdgcn_sched_barrier(0);
  int lane2 = lane;
  asm volatile("" : "+v"(lane2));
  const int c2 = lane2 & 15, g2 = lane2 >> 4, qp2 = (lane2 & 15) >> 2, p2 = lane2 & 3;
  const int fl2 = qp2 | ((g2 & 1) << 2);
  STAMP(19);
  bf16x8 qf[4][3];
#pragma unroll
  for (int s = 0; s < 4; ++s)
#pragma unroll
    for (int t = 0; t < 3; ++t) {
      u32x4 r;
#pragma unroll
      for (int e = 0; e < 2; ++e) {
        const unsigned a = pack2(qacc[2 * s][t][2 * e], qacc[2 * s][t][2 * e + 1]);
        const unsigned bq = pack2(qacc[2 * s + 1][t][2 * e], qacc[2 * s + 1][t][2 * e + 1]);
        auto r1 = __builtin_amdgcn_permlane32_swap(a, bq, false, false);
        auto r2 = __builtin_amdgcn_permlane16_swap(r1[0], r1[1], false, false);
        r[e] = r2[0];
        r[2 + e] = r2[1];
      }
      qf[s][t] = __builtin_bit_cast(bf16x8, r);
    }

  STAMP(20);
  STAMP(21);

  f32x4 sacc[6][3];
#pragma unroll
  for (int kt = 0; kt < 6; ++kt)
#pragma unroll
    for (int t = 0; t < 3; ++t) sacc[kt][t] = (f32x4){0.f, 0.f, 0.f, 0.f};
#pragma unroll
  for (int pc = 0; pc < 3; ++pc) {
    const int kblk = pc < 2 ? 12 + pc * 4 + u : ((0x7632 >> (4 * u)) & 15);
    const unsigned kb = lds0 + kblk * 8192 + c2 * 256;
#pragma unroll
    for (int k2 = 0; k2 < 2; ++k2) {
#pragma unroll
      for (int s = 0; s < 4; ++s) {
        const bf16x8 kf = lds_frag(kb + k2 * 4096 + (((4 * s + g2) ^ c2) << 4));
#pragma unroll
        for (int t = 0; t < 3; ++t) sacc[pc * 2 + k2][t] = MFMA16(kf, qf[s][t], sacc[pc * 2 + k2][t]);
      }
    }
  }
  STAMP(22);
  raw_barrier();
  if (!wx_role) {
    wait_vm<16>();
#pragma unroll
    for (int n = 0; n < 8; ++n) {
      pin(vst[n]);
      lds_store16(lds0 + (12 + (n >> 1)) * 8192 + (wr * 2 + (n & 1)) * 1024 + lane2 * 16, vst[n]);
    }
  } else {
    dma4((unsigned)(((xcd & 3) * 32 + idx) * 32768 + wr * 8192 + lane2 * 128), wo, lds0 + 19 * 8192 + wr * 2048);
  }
  STAMP(23);

  const float cexp = 0.08838834764831845f * 1.4426950408889634f;
  bf16x8 pf[3][3];
  float rinv[3];
#pragma unroll
  for (int t = 0; t < 3; ++t) {
    float m = sacc[0][t][0];
#pragma unroll
    for (int kt = 0; kt < 6; ++kt)
#pragma unroll
      for (int r = 0; r < 4; ++r) m = fmaxf(m, sacc[kt][t][r]);
    m = xg_max(m);
    const float mc = m * cexp;
    float sum = 0.f;
#pragma unroll
    for (int kt = 0; kt < 6; ++kt)
#pragma unroll
      for (int r = 0; r < 4; ++r) {
        const float e = __builtin_amdgcn_exp2f(fmaf(sacc[kt][t][r], cexp, -mc));
        sacc[kt][t][r] = e;
        sum += e;
      }
    sum = xg_sum(sum);
    rinv[t] = 1.0f / sum;
#pragma unroll
    for (int s = 0; s < 3; ++s) {
      u32x4 r;
      r[0] = pack2(sacc[2 * s][t][0], sacc[2 * s][t][1]);
      r[1] = pack2(sacc[2 * s][t][2], sacc[2 * s][t][3]);
      r[2] = pack2(sacc[2 * s + 1][t][0], sacc[2 * s + 1][t][1]);
      r[3] = pack2(sacc[2 * s + 1][t][2], sacc[2 * s + 1][t][3]);
      pf[s][t] = __builtin_bit_cast(bf16x8, r);
    }
  }

  STAMP(24);
  f32x4 oacc[8][3];
#pragma unroll
  for (int dt = 0; dt < 8; ++dt)
#pragma unroll
    for (int t = 0; t < 3; ++t) oacc[dt][t] = (f32x4){0.f, 0.f, 0.f, 0.f};
  const unsigned vlane = lds0 + (4 * g2 + qp2) * 256 + 8 * p2;
#pragma unroll
  for (int si = 0; si < 3; ++si) {
    const int s = si == 0 ? 2 : si - 1;
    if (!wx_role) {
      if (si == 1) wait_vm<8>();
      if (si == 2) wait_vm<0>();
    }
    raw_barrier();
    STAMP(25 + si);
    const int vblk = s == 0 ? ((0x0548 >> (4 * u)) & 15) : s == 1 ? ((0xBA91 >> (4 * u)) & 15) : 12 + u;
    const unsigned vb = vlane + vblk * 8192;
#pragma unroll
    for (int dt = 0; dt < 8; ++dt) {
      const unsigned a0 = vb + ((dt ^ fl2) << 5);
      const bf16x8 vf = cat8(tr_read(a0), tr_read(a0 + 16 * 256));
#pragma unroll
      for (int t = 0; t < 3; ++t) oacc[dt][t] = MFMA16(vf, pf[s][t], oacc[dt][t]);
    }
  }

  STAMP(28);
  u32x4* ow = o_ws + ((((size_t)(blk * NB + b) * HQ + h) * 4) * 6 + qh * 3) * 64 + lane2;
#pragma unroll
  for (int s = 0; s < 4; ++s)
#pragma unroll
    for (int t = 0; t < 3; ++t) {
      const float ri = rinv[t];
      u32x4 r;
      r[0] = pack2(oacc[2 * s][t][0] * ri, oacc[2 * s][t][1] * ri);
      r[1] = pack2(oacc[2 * s][t][2] * ri, oacc[2 * s][t][3] * ri);
      r[2] = pack2(oacc[2 * s + 1][t][0] * ri, oacc[2 * s + 1][t][1] * ri);
      r[3] = pack2(oacc[2 * s + 1][t][2] * ri, oacc[2 * s + 1][t][3] * ri);
      ow[(s * 6 + t) * 64] = r;
    }
  STAMP(29);
}

constexpr int K2_LDS = 65536;

__global__ __launch_bounds__(512, 2) void oproj_kernel(const u32x4* __restrict__ o_ws,
                                                       const short* __restrict__ woA,
                                                       const short* __restrict__ woB,
                                                       unsigned short* __restrict__ out) {
  extern __shared__ __attribute__((aligned(16))) char smem[];
  const int tid = threadIdx.x;
  const int lane = tid & 63;
  const int wave = __builtin_amdgcn_readfirstlane(tid >> 6);
  const int c = lane & 15, g = lane >> 4, qp = (lane & 15) >> 2, p = lane & 3;
  const int bid = blockIdx.x;
  const int xcd = bid & 7, idx = bid >> 3;
  const int blk = xcd >> 2;
  const int b = (xcd & 3) * 4 + (idx >> 3);
  const int nt = idx & 7;
  const int n0 = nt * 64;
  const short* __restrict__ wo = blk ? woB : woA;

  const unsigned lds0 = (unsigned)(uintptr_t)smem;
  char* wbuf = smem + wave * 8192;
  const unsigned ldsW = lds0 + wave * 8192;

  const int srow = lane >> 3, sch = lane & 7;
  const unsigned st_off = srow * 128 + ((((sch >> 1) ^ ((srow >> 1) & 3)) << 5) | ((sch & 1) << 4));
  const short* wsrc = wo + (size_t)(wave * 512 + srow) * DM + n0 + sch * 8;
  const int fl = (2 * g + (qp >> 1)) & 3;
  unsigned wrd[4];
#pragma unroll
  for (int j = 0; j < 4; ++j) wrd[j] = ldsW + (4 * g + qp) * 128 + 8 * p + ((j ^ fl) << 5);

  const u32x4* osrc = o_ws + (((size_t)(blk * NB + b) * HQ + wave * 4) * 4) * 6 * 64 + lane;

  f32x4 acc[4][6];
#pragma unroll
  for (int j = 0; j < 4; ++j)
#pragma unroll
    for (int t = 0; t < 6; ++t) acc[j][t] = (f32x4){0.f, 0.f, 0.f, 0.f};

  u32x4 wst[4];
  u32x4 of[2][6];
#pragma unroll
  for (int i = 0; i < 4; ++i) wst[i] = *(const u32x4*)(wsrc + (size_t)(8 * i) * DM);
#pragma unroll
  for (int t = 0; t < 6; ++t) of[0][t] = osrc[t * 64];
#pragma unroll
  for (int i = 0; i < 4; ++i) *(u32x4*)(wbuf + st_off + i * 8 * 128) = wst[i];
#pragma unroll
  for (int i = 0; i < 4; ++i) wst[i] = *(const u32x4*)(wsrc + (size_t)(32 + 8 * i) * DM);

#pragma unroll 2
  for (int ks = 0; ks < 16; ++ks) {
    if (ks + 1 < 16) {
#pragma unroll
      for (int i = 0; i < 4; ++i) *(u32x4*)(wbuf + ((ks + 1) & 1) * 4096 + st_off + i * 8 * 128) = wst[i];
    }
    if (ks + 2 < 16) {
#pragma unroll
      for (int i = 0; i < 4; ++i) wst[i] = *(const u32x4*)(wsrc + (size_t)(32 * (ks + 2) + 8 * i) * DM);
    }
    if (ks + 1 < 16) {
#pragma unroll
      for (int t = 0; t < 6; ++t) of[(ks + 1) & 1][t] = osrc[((ks + 1) * 6 + t) * 64];
    }
    __builtin_amdgcn_sched_barrier(0);
#pragma unroll
    for (int j = 0; j < 4; ++j) {
      const unsigned a0 = wrd[j] + (ks & 1) * 4096;
      const bf16x8 af = cat8(tr_read(a0), tr_read(a0 + 16 * 128));
#pragma unroll
      for (int t = 0; t < 6; ++t) acc[j][t] = MFMA16(af, __builtin_bit_cast(bf16x8, of[ks & 1][t]), acc[j][t]);
    }
  }

  f32x4* red = (f32x4*)smem;
#pragma unroll
  for (int rd = 0; rd < 3; ++rd) {
    __syncthreads();
#pragma unroll
    for (int j = 0; j < 4; ++j)
#pragma unroll
      for (int tt = 0; tt < 2; ++tt) red[(wave * 8 + j * 2 + tt) * 64 + lane] = acc[j][rd * 2 + tt];
    __syncthreads();
    f32x4 s = red[tid];
#pragma unroll
    for (int w = 1; w < 8; ++w) s += red[w * 512 + tid];
    const int j = wave >> 1, tt = wave & 1;
    const int q = (rd * 2 + tt) * 16 + c;
    const int n = n0 + j * 16 + 4 * g;
    u32x2 r;
    r[0] = pack2(s[0], s[1]);
    r[1] = pack2(s[2], s[3]);
    *(u32x2*)(out + (size_t)(b * SQ + q) * 1024 + blk * 512 + n) = r;
  }
}

}

extern "C" void kernel_launch(void* const* d_in, const int* in_sizes, int n_in, void* d_out, int out_size,
                              void* d_ws, size_t ws_size, hipStream_t stream) {
  const short* xA = (const short*)d_in[0];
  const short* kA = (const short*)d_in[1];
  const short* vA = (const short*)d_in[2];
  const short* wqA = (const short*)d_in[3];
  const short* woA = (const short*)d_in[4];
  const short* xB = (const short*)d_in[5];
  const short* kB = (const short*)d_in[6];
  const short* vB = (const short*)d_in[7];
  const short* wqB = (const short*)d_in[8];
  const short* woB = (const short*)d_in[9];
  u32x4* o_ws = (u32x4*)d_ws;
  (void)hipFuncSetAttribute((const void*)qattn_kernel, hipFuncAttributeMaxDynamicSharedMemorySize, K1_LDS);
  (void)hipFuncSetAttribute((const void*)oproj_kernel, hipFuncAttributeMaxDynamicSharedMemorySize, K2_LDS);
  hipLaunchKernelGGL(qattn_kernel, dim3(256), dim3(512), K1_LDS, stream, xA, kA, vA, wqA, woA, xB, kB, vB, wqB,
                     woB, o_ws);
  hipLaunchKernelGGL(oproj_kernel, dim3(256), dim3(512), K2_LDS, stream, (const u32x4*)o_ws, woA, woB,
                     (unsigned short*)d_out);
}
```

```cpp
#include <hip/hip_runtime.h>
#include <hip/hip_bf16.h>
#include <stdint.h>

typedef __attribute__((ext_vector_type(8))) short bf16x8;
typedef __attribute__((ext_vector_type(4))) short s16x4;
typedef __attribute__((ext_vector_type(4))) float f32x4;
typedef __attribute__((ext_vector_type(2))) float f32x2;
typedef __attribute__((ext_vector_type(4))) unsigned u32x4;
typedef __attribute__((ext_vector_type(2))) unsigned u32x2;
typedef __attribute__((ext_vector_type(2))) __bf16 bf16x2_t;
typedef __attribute__((address_space(3))) s16x4* lds_s16x4_ptr;

#define DI __device__ __forceinline__
#define MFMA16(a, b, c) __builtin_amdgcn_mfma_f32_16x16x32_bf16((a), (b), (c), 0, 0, 0)

#define STAMP(i)
namespace {

constexpr int NB = 16, SQ = 96, DM = 512, HQ = 32, DH = 128, HD = HQ * DH;

DI unsigned pack2(float a, float b) {
  f32x2 v = {a, b};
  return __builtin_bit_cast(unsigned, __builtin_convertvector(v, bf16x2_t));
}
DI s16x4 tr_read(unsigned lds_addr) {
  return __builtin_amdgcn_ds_read_tr16_b64_v4i16((lds_s16x4_ptr)(uintptr_t)lds_addr);
}
DI bf16x8 cat8(s16x4 lo, s16x4 hi) { return __builtin_shufflevector(lo, hi, 0, 1, 2, 3, 4, 5, 6, 7); }
DI float xg_max(float x) {
  auto s = __builtin_amdgcn_permlane16_swap(__float_as_uint(x), __float_as_uint(x), false, false);
  x = fmaxf(__uint_as_float(s[0]), __uint_as_float(s[1]));
  auto t = __builtin_amdgcn_permlane32_swap(__float_as_uint(x), __float_as_uint(x), false, false);
  return fmaxf(__uint_as_float(t[0]), __uint_as_float(t[1]));
}
DI float xg_sum(float x) {
  auto s = __builtin_amdgcn_permlane16_swap(__float_as_uint(x), __float_as_uint(x), false, false);
  x = __uint_as_float(s[0]) + __uint_as_float(s[1]);
  auto t = __builtin_amdgcn_permlane32_swap(__float_as_uint(x), __float_as_uint(x), false, false);
  return __uint_as_float(t[0]) + __uint_as_float(t[1]);
}

constexpr int K1_LDS = 160 * 1024;
constexpr int K1_XBASE = 48 * 1024;
#define KV_NT 1

constexpr int n_w_at(int s) { return (s + 3 <= 15) ? 4 : 0; }
constexpr int n_x_at(int s) { return ((s & 1) && ((s + 3) / 2 <= 7)) ? 6 : 0; }
constexpr int issued_through(int s) {
  int n = 24;
  for (int j = 0; j <= s; ++j) n += n_w_at(j) + n_x_at(j);
  return n;
}
constexpr int pos_w(int st) {
  return st == 0 ? 4 : st == 1 ? 14 : st == 2 ? 18 : issued_through(st - 4) + 4;
}
constexpr int pos_x(int j) { return j == 0 ? 10 : j == 1 ? 24 : issued_through(2 * j - 3); }
constexpr int cmax(int a, int b) { return a > b ? a : b; }
constexpr int wait_before_barrier(int s) {
  return issued_through(s - 1) - cmax(pos_w(s + 1), pos_x((s + 1) >> 1));
}

template <int N> DI void wait_vm() { asm volatile("s_waitcnt vmcnt(%0)" ::"n"(N) : "memory"); }
DI void raw_barrier() {
  asm volatile("s_waitcnt lgkmcnt(0)" ::: "memory");
  __builtin_amdgcn_s_barrier();
  asm volatile("" ::: "memory");
}
DI void dma16(unsigned voff, const void* sbase, unsigned lds_dst) {
  unsigned keep;
  asm volatile("s_mov_b32 %0, m0\n\ts_mov_b32 m0, %3\n\ts_nop 0\n\tglobal_load_lds_dwordx4 %1, %2\n\ts_mov_b32 m0, %0"
               : "=&s"(keep) : "v"(voff), "s"(sbase), "s"(lds_dst) : "memory");
}
DI void dma16_nt(unsigned voff, const void* sbase, unsigned lds_dst) {
  unsigned keep;
  asm volatile("s_mov_b32 %0, m0\n\ts_mov_b32 m0, %3\n\ts_nop 0\n\tglobal_load_lds_dwordx4 %1, %2 nt\n\ts_mov_b32 m0, %0"
               : "=&s"(keep) : "v"(voff), "s"(sbase), "s"(lds_dst) : "memory");
}
DI void dma4(unsigned voff, const void* sbase, unsigned lds_dst) {
  unsigned keep;
  asm volatile("s_mov_b32 %0, m0\n\ts_mov_b32 m0, %3\n\ts_nop 0\n\tglobal_load_lds_dword %1, %2\n\ts_mov_b32 m0, %0"
               : "=&s"(keep) : "v"(voff), "s"(sbase), "s"(lds_dst) : "memory");
}
DI void mfma_acc(f32x4& c, bf16x8 a, bf16x8 b) {
  asm volatile("v_mfma_f32_16x16x32_bf16 %0, %1, %2, %0" : "+v"(c) : "v"(a), "v"(b));
}
DI void mfma_init(f32x4& c, bf16x8 a, bf16x8 b) {
  asm volatile("v_mfma_f32_16x16x32_bf16 %0, %1, %2, 0" : "=&v"(c) : "v"(a), "v"(b));
}
template <int V> struct IC { static constexpr int value = V; };
typedef const __attribute__((address_space(3))) bf16x8* lds_frag_ptr;
DI bf16x8 lds_frag(unsigned a) { return *(lds_frag_ptr)(uintptr_t)a; }

__global__ __launch_bounds__(512, 2) void qattn_kernel(
    const short* __restrict__ xA, const short* __restrict__ kA, const short* __restrict__ vA,
    const short* __restrict__ wqA, const short* __restrict__ woA, const short* __restrict__ xB,
    const short* __restrict__ kB, const short* __restrict__ vB, const short* __restrict__ wqB,
    const short* __restrict__ woB, u32x4* __restrict__ o_ws) {
  extern __shared__ __attribute__((aligned(16))) char smem[];
  const int tid = threadIdx.x;
  const int lane = tid & 63;
  const int wave = __builtin_amdgcn_readfirstlane(tid >> 6);
  const int c = lane & 15, g = lane >> 4, qp = (lane & 15) >> 2, p = lane & 3;
  const int bid = blockIdx.x;
  const int xcd = bid & 7, idx = bid >> 3;
  const int blk = xcd >> 2;
  const int h0 = ((xcd & 3) * 4 + (idx & 3)) * 2;
  const int b0 = (idx >> 2) * 2;
  const int u = wave >> 1, qh = wave & 1;
  const int bb = u >> 1, hh = u & 1;
  const int b = b0 + bb, h = h0 + hh;
  const bool wx_role = wave < 4;
  const int wr = wave & 3;

  const char* __restrict__ x = (const char*)(blk ? xB : xA) + (size_t)b0 * SQ * DM * 2;
  const char* __restrict__ kc = (const char*)(blk ? kB : kA) + ((size_t)b0 * HQ + h0) * (SQ * DH * 2);
  const char* __restrict__ vc = (const char*)(blk ? vB : vA) + ((size_t)b0 * HQ + h0) * (SQ * DH * 2);
  const char* __restrict__ wq = (const char*)(blk ? wqB : wqA) + h0 * DH * 2;
  const char* __restrict__ wo = (const char*)(blk ? woB : woA);

  const unsigned lds0 = (unsigned)(uintptr_t)smem;
  STAMP(0);

  unsigned voff[4];
  if (wx_role) {
#pragma unroll
    for (int e = 0; e < 2; ++e) {
      const int f = g | (e << 2);
      const int ch = (((c >> 1) ^ f) << 1) | (c & 1);
      voff[e] = (unsigned)((16 * (wr & 1) + g) * (HD * 2) + (wr >> 1) * (DH * 2) + ch * 16);
      const int chx = (lane & 7) ^ ((e << 2) | g);
      voff[2 + e] = (unsigned)((lane >> 3) * (DM * 2) + chx * 16);
    }
  } else {
#pragma unroll
    for (int e = 0; e < 2; ++e) {
      const int row = 4 * (2 * wr + e) + g;
      voff[e] = (unsigned)(row * 256 + ((c ^ (row & 15)) << 4));
      voff[2 + e] = (unsigned)(row * 256 + ((((((c >> 1) ^ (row & 7)) << 1) | (c & 1))) << 4));
    }
  }

  auto issue_w = [&](int st) {
#pragma unroll
    for (int e = 0; e < 4; ++e)
      dma16(voff[e >> 1], wq + (size_t)(st * 32 + 4 * e) * (HD * 2), lds0 + (st % 3) * 16384 + (wr * 4 + e) * 1024);
  };
  auto issue_x = [&](int j) {
#pragma unroll
    for (int e = 0; e < 6; ++e)
      dma16(voff[2 + (e & 1)], x + (size_t)(wr * 48 + 8 * e) * (DM * 2) + j * 128,
            lds0 + K1_XBASE + (j & 1) * 24576 + (wr * 6 + e) * 1024);
  };
  auto issue_kv = [&](int kind, int uu, int pc, int blkidx) {
    const char* src = (kind ? vc : kc) + ((size_t)(uu >> 1) * HQ + (uu & 1)) * (SQ * DH * 2) + pc * 8192;
#pragma unroll
    for (int e = 0; e < 2; ++e) dma16_nt(voff[2 * kind + e], src, lds0 + blkidx * 8192 + (wr * 2 + e) * 1024);
  };

  const int fl = qp | ((g & 1) << 2);
  unsigned trw[8];
#pragma unroll
  for (int dt = 0; dt < 8; ++dt) trw[dt] = lds0 + hh * 8192 + (8 * g + qp) * 256 + 8 * p + ((dt ^ fl) << 5);
  unsigned xrd[2];
#pragma unroll
  for (int hf = 0; hf < 2; ++hf)
    xrd[hf] = lds0 + K1_XBASE + (bb * SQ + qh * 48 + c) * 128 + (((4 * hf + g) ^ ((c >> 1) & 7)) << 4);

  f32x4 qacc[8][3];
  bf16x8 af[8], xf[3];
  auto read_a = [&](int st, int dt) {
    const unsigned a0 = trw[dt] + (st % 3) * 16384;
    return cat8(tr_read(a0), tr_read(a0 + 4 * 256));
  };
  auto read_x = [&](int st, int t) { return lds_frag(xrd[st & 1] + ((st >> 1) & 1) * 24576 + t * 16 * 128); };

  if (wx_role) {
    issue_w(0); issue_x(0); issue_w(1); issue_w(2); issue_x(1);
    wait_vm<14>();
  } else {
    const unsigned dump = lds0 + 19 * 8192 + wr * 2048;
    dma4((unsigned)(((idx >> 2) * 64 + wr * 16 + (lane >> 2)) * (HD * 2) + (lane & 3) * 128), wq, dump);
    dma4((unsigned)(((idx & 3) * 48 + wr * 12) * (DM * 2) + lane * 128), x, dump + 256);
    dma4((unsigned)(((idx & 3) * 48 + wr * 12 + 8) * (DM * 2) + (lane & 31) * 128), x, dump + 512);
    issue_kv(0, 0, 0, 12); issue_kv(0, 1, 0, 13);
  }
  raw_barrier();
#pragma unroll
  for (int t = 0; t < 3; ++t) xf[t] = read_x(0, t);
#pragma unroll
  for (int dt = 0; dt < 8; ++dt) af[dt] = read_a(0, dt);

  auto step = [&](auto ic) {
    constexpr int s = decltype(ic)::value;
    STAMP(1 + s);
    if (wx_role) {
      if constexpr (s < 15) wait_vm<wait_before_barrier(s)>();
    }
    raw_barrier();
    if (wx_role) {
      if constexpr (n_w_at(s) > 0) issue_w(s + 3);
      if constexpr (n_x_at(s) > 0) issue_x((s + 3) / 2);
    } else {
      if constexpr (s < 6) issue_kv(0, (s + 2) & 3, (s + 2) >> 2, 12 + s + 2);
      if constexpr (s == 13) { issue_kv(0, 0, 2, 2); issue_kv(0, 1, 2, 3); issue_kv(0, 2, 2, 6); issue_kv(0, 3, 2, 7); issue_kv(1, 0, 0, 8); }
      if constexpr (s == 14) { issue_kv(1, 1, 0, 4); issue_kv(1, 2, 0, 5); }
      if constexpr (s == 15) { issue_kv(1, 3, 0, 0); issue_kv(1, 0, 1, 1); issue_kv(1, 1, 1, 9); issue_kv(1, 2, 1, 10); issue_kv(1, 3, 1, 11); }
    }
    bf16x8 xn[3], an[8];
    if constexpr (s < 15) {
#pragma unroll
      for (int t = 0; t < 3; ++t) xn[t] = read_x(s + 1, t);
      an[0] = read_a(s + 1, 0);
      an[1] = read_a(s + 1, 1);
    }
    __builtin_amdgcn_sched_barrier(0);
#pragma unroll
    for (int dt = 0; dt < 8; ++dt) {
#pragma unroll
      for (int t = 0; t < 3; ++t) {
        if constexpr (s == 0) mfma_init(qacc[dt][t], af[dt], xf[t]);
        else mfma_acc(qacc[dt][t], af[dt], xf[t]);
      }
      if constexpr (s < 15) {
        if (dt + 2 < 8) an[dt + 2] = read_a(s + 1, dt + 2);
      }
      __builtin_amdgcn_sched_barrier(0);
    }
    if constexpr (s < 15) {
#pragma unroll
      for (int t = 0; t < 3; ++t) xf[t] = xn[t];
#pragma unroll
      for (int dt = 0; dt < 8; ++dt) af[dt] = an[dt];
    }
  };
  step(IC<0>{}); step(IC<1>{}); step(IC<2>{}); step(IC<3>{}); step(IC<4>{}); step(IC<5>{}); step(IC<6>{}); step(IC<7>{});
  step(IC<8>{}); step(IC<9>{}); step(IC<10>{}); step(IC<11>{}); step(IC<12>{}); step(IC<13>{}); step(IC<14>{}); step(IC<15>{});

  asm volatile("s_nop 7\n\ts_nop 7" ::: "memory");
  __builtin_amdgcn_sched_barrier(0);
  STAMP(17);
  bf16x8 qf[4][3];
#pragma unroll
  for (int s = 0; s < 4; ++s)
#pragma unroll
    for (int t = 0; t < 3; ++t) {
      u32x4 r;
#pragma unroll
      for (int e = 0; e < 2; ++e) {
        const unsigned a = pack2(qacc[2 * s][t][2 * e], qacc[2 * s][t][2 * e + 1]);
        const unsigned bq = pack2(qacc[2 * s + 1][t][2 * e], qacc[2 * s + 1][t][2 * e + 1]);
        auto r1 = __builtin_amdgcn_permlane32_swap(a, bq, false, false);
        auto r2 = __builtin_amdgcn_permlane16_swap(r1[0], r1[1], false, false);
        r[e] = r2[0];
        r[2 + e] = r2[1];
      }
      qf[s][t] = __builtin_bit_cast(bf16x8, r);
    }

  if (!wx_role) wait_vm<16>();
  raw_barrier();

  f32x4 sacc[6][3];
#pragma unroll
  for (int kt = 0; kt < 6; ++kt)
#pragma unroll
    for (int t = 0; t < 3; ++t) sacc[kt][t] = (f32x4){0.f, 0.f, 0.f, 0.f};
#pragma unroll
  for (int pc = 0; pc < 3; ++pc) {
    const int kblk = pc < 2 ? 12 + pc * 4 + u : ((0x7632 >> (4 * u)) & 15);
    const unsigned kb = lds0 + kblk * 8192 + c * 256;
#pragma unroll
    for (int k2 = 0; k2 < 2; ++k2) {
#pragma unroll
      for (int s = 0; s < 4; ++s) {
        const bf16x8 kf = lds_frag(kb + k2 * 4096 + (((4 * s + g) ^ c) << 4));
#pragma unroll
        for (int t = 0; t < 3; ++t) sacc[pc * 2 + k2][t] = MFMA16(kf, qf[s][t], sacc[pc * 2 + k2][t]);
      }
    }
  }
  STAMP(18);
  raw_barrier();
  if (!wx_role) {
    issue_kv(1, 0, 2, 12); issue_kv(1, 1, 2, 13); issue_kv(1, 2, 2, 14); issue_kv(1, 3, 2, 15);
    dma4((unsigned)(((xcd & 3) * 32 + idx) * 32768 + wr * 8192 + lane * 128), wo, lds0 + 19 * 8192 + wr * 2048);
  }

  const float cexp = 0.08838834764831845f * 1.4426950408889634f;
  bf16x8 pf[3][3];
  float rinv[3];
#pragma unroll
  for (int t = 0; t < 3; ++t) {
    float m = sacc[0][t][0];
#pragma unroll
    for (int kt = 0; kt < 6; ++kt)
#pragma unroll
      for (int r = 0; r < 4; ++r) m = fmaxf(m, sacc[kt][t][r]);
    m = xg_max(m);
    const float mc = m * cexp;
    float sum = 0.f;
#pragma unroll
    for (int kt = 0; kt < 6; ++kt)
#pragma unroll
      for (int r = 0; r < 4; ++r) {
        const float e = __builtin_amdgcn_exp2f(fmaf(sacc[kt][t][r], cexp, -mc));
        sacc[kt][t][r] = e;
        sum += e;
      }
    sum = xg_sum(sum);
    rinv[t] = 1.0f / sum;
#pragma unroll
    for (int s = 0; s < 3; ++s) {
      u32x4 r;
      r[0] = pack2(sacc[2 * s][t][0], sacc[2 * s][t][1]);
      r[1] = pack2(sacc[2 * s][t][2], sacc[2 * s][t][3]);
      r[2] = pack2(sacc[2 * s + 1][t][0], sacc[2 * s + 1][t][1]);
      r[3] = pack2(sacc[2 * s + 1][t][2], sacc[2 * s + 1][t][3]);
      pf[s][t] = __builtin_bit_cast(bf16x8, r);
    }
  }

  STAMP(19);
  f32x4 oacc[8][3];
#pragma unroll
  for (int dt = 0; dt < 8; ++dt)
#pragma unroll
    for (int t = 0; t < 3; ++t) oacc[dt][t] = (f32x4){0.f, 0.f, 0.f, 0.f};
  const unsigned vlane = lds0 + (4 * g + qp) * 256 + 8 * p;
#pragma unroll
  for (int s = 0; s < 3; ++s) {
    if (!wx_role) {
      if (s == 0) wait_vm<17>();
      if (s == 1) wait_vm<9>();
      if (s == 2) wait_vm<1>();
    }
    raw_barrier();
    const int vblk = s == 0 ? ((0x0548 >> (4 * u)) & 15) : s == 1 ? ((0xBA91 >> (4 * u)) & 15) : 12 + u;
    const unsigned vb = vlane + vblk * 8192;
#pragma unroll
    for (int dt = 0; dt < 8; ++dt) {
      const unsigned a0 = vb + ((dt ^ fl) << 5);
      const bf16x8 vf = cat8(tr_read(a0), tr_read(a0 + 16 * 256));
#pragma unroll
      for (int t = 0; t < 3; ++t) oacc[dt][t] = MFMA16(vf, pf[s][t], oacc[dt][t]);
    }
  }

  STAMP(20);
  u32x4* ow = o_ws + ((((size_t)(blk * NB + b) * HQ + h) * 4) * 6 + qh * 3) * 64 + lane;
#pragma unroll
  for (int s = 0; s < 4; ++s)
#pragma unroll
    for (int t = 0; t < 3; ++t) {
      const float ri = rinv[t];
      u32x4 r;
      r[0] = pack2(oacc[2 * s][t][0] * ri, oacc[2 * s][t][1] * ri);
      r[1] = pack2(oacc[2 * s][t][2] * ri, oacc[2 * s][t][3] * ri);
      r[2] = pack2(oacc[2 * s + 1][t][0] * ri, oacc[2 * s + 1][t][1] * ri);
      r[3] = pack2(oacc[2 * s + 1][t][2] * ri, oacc[2 * s + 1][t][3] * ri);
      ow[(s * 6 + t) * 64] = r;
    }
  STAMP(21);
}

constexpr int K2_LDS = 128 * 1024;

constexpr int k2_n(int sg, int t) { return ((sg + 3 <= 15) ? 1 : 0) + ((t < 4 && sg + 4 <= 15) ? 1 : 0); }
constexpr int k2_before(int sg, int t) {
  int n = 4;
  for (int a = -3; a < sg; ++a)
    for (int b = 0; b < 6; ++b) n += k2_n(a, b);
  for (int b = 0; b < t; ++b) n += k2_n(sg, b);
  return n;
}
constexpr int k2_wait(int s, int t) {
  int w = k2_before(s, t) - (k2_before(s - 3, t) + 1);
  if (t == 4 && s < 15) {
    const int w2 = k2_before(s, 4) - (k2_before(s - 3, 3) + 2);
    if (w2 < w) w = w2;
  }
  return w;
}
template <int OFF> DI void gload16(u32x4& d, unsigned voff, const void* sbase) {
  asm volatile("global_load_dwordx4 %0, %1, %2 offset:%3" : "=v"(d) : "v"(voff), "s"(sbase), "n"(OFF) : "memory");
}
DI void mfma_acc_u(f32x4& c, bf16x8 a, u32x4 b) {
  asm volatile("v_mfma_f32_16x16x32_bf16 %0, %1, %2, %0" : "+v"(c) : "v"(a), "v"(b));
}
DI void mfma_init_u(f32x4& c, bf16x8 a, u32x4 b) {
  asm volatile("v_mfma_f32_16x16x32_bf16 %0, %1, %2, 0" : "=&v"(c) : "v"(a), "v"(b));
}

__global__ __launch_bounds__(512, 2) void oproj_kernel(const u32x4* __restrict__ o_ws,
                                                       const short* __restrict__ woA,
                                                       const short* __restrict__ woB,
                                                       unsigned short* __restrict__ out) {
  extern __shared__ __attribute__((aligned(16))) char smem[];
  const int tid = threadIdx.x;
  const int lane = tid & 63;
  const int wave = __builtin_amdgcn_readfirstlane(tid >> 6);
  const int c = lane & 15, g = lane >> 4, qp = (lane & 15) >> 2, p = lane & 3;
  const int bid = blockIdx.x;
  const int xcd = bid & 7, idx = bid >> 3;
  const int blk = xcd >> 2;
  const int b = (xcd & 3) * 4 + (idx >> 3);
  const int n0 = (idx & 7) * 64;
  const int jw = wave & 3, tw = (wave >> 2) * 3;

  const unsigned lds0 = (unsigned)(uintptr_t)smem;
  const unsigned ldsW = lds0 + wave * 16384;

  const int srow = lane >> 3, pos = lane & 7;
  const unsigned wvoff = (unsigned)(srow * (DM * 2) + (((((pos >> 1) ^ ((srow >> 1) & 3)) << 1) | (pos & 1)) << 4));
  const char* wbase = (const char*)(blk ? woB : woA) + ((size_t)(wave * 512) * DM + n0) * 2;
  const int fl = (2 * g + (qp >> 1)) & 3;
  unsigned wrd[4];
#pragma unroll
  for (int j = 0; j < 4; ++j) wrd[j] = ldsW + (4 * g + qp) * 128 + 8 * p + (((j ^ jw) ^ fl) << 5);

  const char* obase = (const char*)(o_ws + (((size_t)(blk * NB + b) * HQ + wave * 4) * 4) * 6 * 64);
  const unsigned ovoff0 = (unsigned)(lane * 16 + tw * 1024), ovoff1 = (unsigned)(lane * 16 + (3 - tw) * 1024);

  f32x4 acc[4][6];
  u32x4 of[3][6];
  bf16x8 af[4];

  auto issue_w = [&](int st, int e) {
    dma16(wvoff, wbase + (size_t)(st * 32 + 8 * e) * (DM * 2), ldsW + (st & 3) * 4096 + e * 1024);
  };
  auto issue_o = [&](int st, auto tc) {
    constexpr int t = decltype(tc)::value;
    gload16<(t % 3) * 1024>(of[st % 3][t], t < 3 ? ovoff0 : ovoff1, obase + st * 6144);
  };
  auto read_a = [&](int st, int j) {
    const unsigned a0 = wrd[j] + (st & 3) * 4096;
    return cat8(tr_read(a0), tr_read(a0 + 16 * 128));
  };
  auto slot = [&](auto sc, auto tc) {
    constexpr int sg = decltype(sc)::value, t = decltype(tc)::value;
    if constexpr (sg + 3 <= 15) issue_o(sg + 3, tc);
    if constexpr (t < 4 && sg + 4 <= 15) issue_w(sg + 4, t);
  };
  auto slots = [&](auto sc) {
    slot(sc, IC<0>{}); slot(sc, IC<1>{}); slot(sc, IC<2>{}); slot(sc, IC<3>{}); slot(sc, IC<4>{}); slot(sc, IC<5>{});
  };

#pragma unroll
  for (int e = 0; e < 4; ++e) issue_w(0, e);
  slots(IC<-3>{}); slots(IC<-2>{}); slots(IC<-1>{});
  wait_vm<30>();
#pragma unroll
  for (int j = 0; j < 4; ++j) af[j] = read_a(0, j);

  auto group = [&](auto sc, auto tc, bf16x8* an) {
    constexpr int s = decltype(sc)::value, t = decltype(tc)::value;
    wait_vm<k2_wait(s, t)>();
    if constexpr (t == 4 && s < 15) {
#pragma unroll
      for (int j = 0; j < 4; ++j) an[j] = read_a(s + 1, j);
    }
    __builtin_amdgcn_sched_barrier(0);
#pragma unroll
    for (int j = 0; j < 4; ++j) {
      if constexpr (s == 0) mfma_init_u(acc[j][t], af[j], of[s % 3][t]);
      else mfma_acc_u(acc[j][t], af[j], of[s % 3][t]);
    }
    slot(sc, tc);
    __builtin_amdgcn_sched_barrier(0);
  };
  auto step = [&](auto sc) {
    constexpr int s = decltype(sc)::value;
    bf16x8 an[4];
    group(sc, IC<0>{}, an); group(sc, IC<1>{}, an); group(sc, IC<2>{}, an);
    group(sc, IC<3>{}, an); group(sc, IC<4>{}, an); group(sc, IC<5>{}, an);
    if constexpr (s < 15) {
#pragma unroll
      for (int j = 0; j < 4; ++j) af[j] = an[j];
    }
  };
  step(IC<0>{}); step(IC<1>{}); step(IC<2>{}); step(IC<3>{}); step(IC<4>{}); step(IC<5>{}); step(IC<6>{}); step(IC<7>{});
  step(IC<8>{}); step(IC<9>{}); step(IC<10>{}); step(IC<11>{}); step(IC<12>{}); step(IC<13>{}); step(IC<14>{}); step(IC<15>{});
  asm volatile("s_nop 7\n\ts_nop 7" ::: "memory");
  __builtin_amdgcn_sched_barrier(0);

  f32x4* mine = (f32x4*)(smem + wave * 16384) + lane;
  f32x4* part = (f32x4*)(smem + (wave ^ 4) * 16384) + lane;
#pragma unroll
  for (int j = 0; j < 4; ++j)
#pragma unroll
    for (int i = 0; i < 3; ++i) mine[(j * 3 + i) * 64] = acc[j][3 + i];
  __syncthreads();
#pragma unroll
  for (int j = 0; j < 4; ++j)
#pragma unroll
    for (int i = 0; i < 3; ++i) {
      const f32x4 o = part[(j * 3 + i) * 64];
      acc[j][i] = (wave < 4) ? acc[j][i] + o : o + acc[j][i];
    }
  asm volatile("s_waitcnt lgkmcnt(0)" ::: "memory");
#pragma unroll
  for (int j = 1; j < 4; ++j)
#pragma unroll
    for (int i = 0; i < 3; ++i) part[((j - 1) * 3 + i) * 64] = acc[j][i];
  __syncthreads();
  f32x4 s4[4][3];
#pragma unroll
  for (int d = 1; d < 4; ++d) {
    const f32x4* src = (const f32x4*)(smem + ((wave ^ d) ^ 4) * 16384) + lane;
#pragma unroll
    for (int i = 0; i < 3; ++i) s4[d][i] = src[((d - 1) * 3 + i) * 64];
  }
#pragma unroll
  for (int i = 0; i < 3; ++i) {
    const f32x4 s = (acc[0][i] + s4[1][i]) + (s4[2][i] + s4[3][i]);
    const int q = (tw + i) * 16 + c;
    const int n = n0 + jw * 16 + 4 * g;
    u32x2 r;
    r[0] = pack2(s[0], s[1]);
    r[1] = pack2(s[2], s[3]);
    *(u32x2*)(out + (size_t)(b * SQ + q) * 1024 + blk * 512 + n) = r;
  }
}

}

extern "C" void kernel_launch(void* const* d_in, const int* in_sizes, int n_in, void* d_out, int out_size,
                              void* d_ws, size_t ws_size, hipStream_t stream) {
  const short* xA = (const short*)d_in[0];
  const short* kA = (const short*)d_in[1];
  const short* vA = (const short*)d_in[2];
  const short* wqA = (const short*)d_in[3];
  const short* woA = (const short*)d_in[4];
  const short* xB = (const short*)d_in[5];
  const short* kB = (const short*)d_in[6];
  const short* vB = (const short*)d_in[7];
  const short* wqB = (const short*)d_in[8];
  const short* woB = (const short*)d_in[9];
  u32x4* o_ws = (u32x4*)d_ws;
  (void)hipFuncSetAttribute((const void*)qattn_kernel, hipFuncAttributeMaxDynamicSharedMemorySize, K1_LDS);
  (void)hipFuncSetAttribute((const void*)oproj_kernel, hipFuncAttributeMaxDynamicSharedMemorySize, K2_LDS);
  hipLaunchKernelGGL(qattn_kernel, dim3(256), dim3(512), K1_LDS, stream, xA, kA, vA, wqA, woA, xB, kB, vB, wqB,
                     woB, o_ws);
  hipLaunchKernelGGL(oproj_kernel, dim3(256), dim3(512), K2_LDS, stream, (const u32x4*)o_ws, woA, woB,
                     (unsigned short*)d_out);
}
```

```cpp
#include <hip/hip_runtime.h>
#include <hip/hip_bf16.h>
#include <stdint.h>

typedef __attribute__((ext_vector_type(8))) short bf16x8;
typedef __attribute__((ext_vector_type(4))) short s16x4;
typedef __attribute__((ext_vector_type(4))) float f32x4;
typedef __attribute__((ext_vector_type(2))) float f32x2;
typedef __attribute__((ext_vector_type(4))) unsigned u32x4;
typedef __attribute__((ext_vector_type(2))) unsigned u32x2;
typedef __attribute__((ext_vector_type(2))) __bf16 bf16x2_t;
typedef __attribute__((address_space(3))) s16x4* lds_s16x4_ptr;

#define DI __device__ __forceinline__
#define MFMA16(a, b, c) __builtin_amdgcn_mfma_f32_16x16x32_bf16((a), (b), (c), 0, 0, 0)

#define STAMP(i)
namespace {

constexpr int NB = 16, SQ = 96, DM = 512, HQ = 32, DH = 128, HD = HQ * DH;

DI unsigned pack2(float a, float b) {
  f32x2 v = {a, b};
  return __builtin_bit_cast(unsigned, __builtin_convertvector(v, bf16x2_t));
}
DI s16x4 tr_read(unsigned lds_addr) {
  return __builtin_amdgcn_ds_read_tr16_b64_v4i16((lds_s16x4_ptr)(uintptr_t)lds_addr);
}
DI bf16x8 cat8(s16x4 lo, s16x4 hi) { return __builtin_shufflevector(lo, hi, 0, 1, 2, 3, 4, 5, 6, 7); }
DI float xg_max(float x) {
  auto s = __builtin_amdgcn_permlane16_swap(__float_as_uint(x), __float_as_uint(x), false, false);
  x = fmaxf(__uint_as_float(s[0]), __uint_as_float(s[1]));
  auto t = __builtin_amdgcn_permlane32_swap(__float_as_uint(x), __float_as_uint(x), false, false);
  return fmaxf(__uint_as_float(t[0]), __uint_as_float(t[1]));
}
DI float xg_sum(float x) {
  auto s = __builtin_amdgcn_permlane16_swap(__float_as_uint(x), __float_as_uint(x), false, false);
  x = __uint_as_float(s[0]) + __uint_as_float(s[1]);
  auto t = __builtin_amdgcn_permlane32_swap(__float_as_uint(x), __float_as_uint(x), false, false);
  return __uint_as_float(t[0]) + __uint_as_float(t[1]);
}

constexpr int K1_LDS = 160 * 1024;
constexpr int XBASE = 96 * 1024;

constexpr int piece_blk(int i) {
  constexpr int tb[24] = {15, 16, 17, 18, 19, 4, 5, 6, 7, 12, 8, 9,
                          10, 11, 0, 1, 2, 3, 13, 14, 15, 16, 17, 18};
  return tb[i];
}
constexpr unsigned blk_tab(int kind, int pc) {
  unsigned r = 0;
  for (int u = 0; u < 4; ++u) r |= (unsigned)piece_blk(kind * 12 + pc * 4 + u) << (8 * u);
  return r;
}
constexpr int ring_blk(int w) {
  return piece_blk(4 + w);
}
constexpr unsigned long long ring_tab() {
  unsigned long long r = 0;
  for (int w = 0; w < 8; ++w) r |= (unsigned long long)ring_blk(w) << (8 * w);
  return r;
}

constexpr int n_w(int s) { return (s + 3 <= 15) ? 4 : 0; }
constexpr int n_x(int s) { return ((s & 1) && ((s + 3) / 2 <= 7)) ? 2 : 0; }
constexpr int first_piece(int s) {
  return s <= 2 ? 0 : s <= 4 ? 2 : s <= 6 ? 4 : s <= 13 ? 5 : s == 14 ? 10 : s == 15 ? 14 : 20;
}
constexpr int n_p(int s) { return first_piece(s + 1) - first_piece(s); }
constexpr int n_t(int s) { return (s == 0 || s == 8) ? 1 : 0; }
constexpr int cnt(int s) {
  int n = 16;
  for (int j = 0; j <= s; ++j) n += n_w(j) + n_x(j) + n_p(j) + n_t(j);
  return n;
}
constexpr int pos_w(int st) { return st == 0 ? 4 : st == 1 ? 10 : st == 2 ? 14 : cnt(st - 4) + 4; }
constexpr int pos_x(int j) { return j == 0 ? 6 : j == 1 ? 16 : cnt(2 * j - 4) + 6; }
constexpr int pos_p(int i) {
  int s = 0;
  while (first_piece(s + 1) <= i) ++s;
  return cnt(s - 1) + n_w(s) + n_x(s) + (i - first_piece(s)) + 1;
}
constexpr int cmax(int a, int b) { return a > b ? a : b; }
constexpr int step_wait(int s) { return cnt(s - 1) - cmax(pos_w(s + 1), pos_x((s + 1) >> 1)); }

template <int N> DI void wait_vm() { asm volatile("s_waitcnt vmcnt(%0)" ::"n"(N) : "memory"); }
DI void raw_barrier() {
  asm volatile("s_waitcnt lgkmcnt(0)" ::: "memory");
  __builtin_amdgcn_s_barrier();
  asm volatile("" ::: "memory");
}
DI void dma16(unsigned voff, const void* sbase, unsigned lds_dst) {
  unsigned keep;
  asm volatile("s_mov_b32 %0, m0\n\ts_mov_b32 m0, %3\n\ts_nop 0\n\tglobal_load_lds_dwordx4 %1, %2\n\ts_mov_b32 m0, %0"
               : "=&s"(keep) : "v"(voff), "s"(sbase), "s"(lds_dst) : "memory");
}
DI void dma16_nt(unsigned voff, const void* sbase, unsigned lds_dst) {
  unsigned keep;
  asm volatile("s_mov_b32 %0, m0\n\ts_mov_b32 m0, %3\n\ts_nop 0\n\tglobal_load_lds_dwordx4 %1, %2 nt\n\ts_mov_b32 m0, %0"
               : "=&s"(keep) : "v"(voff), "s"(sbase), "s"(lds_dst) : "memory");
}
DI void touch(unsigned voff, const void* sbase) {
  asm volatile("global_load_dword v255, %0, %1" ::"v"(voff), "s"(sbase) : "v255", "memory");
}
DI void mfma_acc(f32x4& c, bf16x8 a, bf16x8 b) {
  asm volatile("v_mfma_f32_16x16x32_bf16 %0, %1, %2, %0" : "+v"(c) : "v"(a), "v"(b));
}
DI void mfma_init(f32x4& c, bf16x8 a, bf16x8 b) {
  asm volatile("v_mfma_f32_16x16x32_bf16 %0, %1, %2, 0" : "=&v"(c) : "v"(a), "v"(b));
}
template <int V> struct IC { static constexpr int value = V; };
typedef const __attribute__((address_space(3))) bf16x8* lds_frag_ptr;
typedef __attribute__((address_space(3))) u32x4* lds_u32x4_ptr;
DI bf16x8 lds_frag(unsigned a) { return *(lds_frag_ptr)(uintptr_t)a; }

__global__ __launch_bounds__(512, 2) void fused_kernel(
    const short* __restrict__ xA, const short* __restrict__ kA, const short* __restrict__ vA,
    const short* __restrict__ wqA, const short* __restrict__ woA, const short* __restrict__ xB,
    const short* __restrict__ kB, const short* __restrict__ vB, const short* __restrict__ wqB,
    const short* __restrict__ woB, u32x4* __restrict__ p_ws) {
  extern __shared__ __attribute__((aligned(16))) char smem[];
  const int tid = threadIdx.x;
  const int lane = tid & 63;
  const int wave = __builtin_amdgcn_readfirstlane(tid >> 6);
  const int c = lane & 15, g = lane >> 4, qp = (lane & 15) >> 2, p = lane & 3;
  const int bid = blockIdx.x;
  const int xcd = bid & 7, idx = bid >> 3;
  const int blk = xcd >> 2;
  const int hg = (xcd & 3) * 2 + (idx & 1);
  const int b = idx >> 1;
  const int h0 = hg * 4;
  const int u = wave >> 1, qh = wave & 1;

  const char* __restrict__ x = (const char*)(blk ? xB : xA) + (size_t)b * SQ * DM * 2;
  const char* __restrict__ kc = (const char*)(blk ? kB : kA) + ((size_t)b * HQ + h0) * (SQ * DH * 2);
  const char* __restrict__ vc = (const char*)(blk ? vB : vA) + ((size_t)b * HQ + h0) * (SQ * DH * 2);
  const char* __restrict__ wq = (const char*)(blk ? wqB : wqA) + h0 * DH * 2;
  const char* __restrict__ wo = (const char*)(blk ? woB : woA) + (size_t)h0 * DH * DM * 2;

  const unsigned lds0 = (unsigned)(uintptr_t)smem;
  STAMP(0);

  unsigned vw[2], vx[2], vk, vv;
#pragma unroll
  for (int e = 0; e < 2; ++e) {
    const int f = g | (e << 2);
    const int ch = (((c >> 1) ^ f) << 1) | (c & 1);
    vw[e] = (unsigned)((16 * qh + g) * (HD * 2) + u * (DH * 2) + ch * 16);
    const int r = 12 * wave + 6 * e + (lane >> 3);
    vx[e] = (unsigned)(r * (DM * 2) + (((lane & 7) ^ ((r >> 1) & 7)) << 4));
  }
  {
    const int row = 4 * wave + g;
    vk = (unsigned)(row * 256 + ((c ^ (row & 15)) << 4));
    vv = (unsigned)(row * 256 + (((((c >> 1) ^ (row & 7)) << 1) | (c & 1)) << 4));
  }

  auto issue_w = [&](int st) {
#pragma unroll
    for (int e = 0; e < 4; ++e)
      dma16(vw[e >> 1], wq + (size_t)(st * 32 + 4 * e) * (HD * 2), lds0 + (st % 3) * 32768 + (wave * 4 + e) * 1024);
  };
  auto issue_x = [&](int j) {
    if (lane < 48) {
#pragma unroll
      for (int e = 0; e < 2; ++e)
        dma16(vx[e], x + j * 128, lds0 + XBASE + (j & 1) * 12288 + (12 * wave + 6 * e) * 128);
    }
  };
  auto issue_piece = [&](int i) {
    const int kind = i / 12, uu = i & 3, pc = (i % 12) >> 2;
    dma16_nt(kind ? vv : vk, (kind ? vc : kc) + uu * (SQ * DH * 2) + pc * 8192, lds0 + piece_blk(i) * 8192 + wave * 1024);
  };

  const int fl = qp | ((g & 1) << 2);
  unsigned trw[8];
#pragma unroll
  for (int dt = 0; dt < 8; ++dt) trw[dt] = lds0 + u * 8192 + (8 * g + qp) * 256 + 8 * p + ((dt ^ fl) << 5);
  unsigned xrd[2];
#pragma unroll
  for (int hf = 0; hf < 2; ++hf)
    xrd[hf] = lds0 + XBASE + (qh * 48 + c) * 128 + (((4 * hf + g) ^ ((c >> 1) & 7)) << 4);

  f32x4 qacc[8][3];
  bf16x8 af[8], xf[3];
  auto read_a = [&](int st, int dt) {
    const unsigned a0 = trw[dt] + (st % 3) * 32768;
    return cat8(tr_read(a0), tr_read(a0 + 4 * 256));
  };
  auto read_x = [&](int st, int t) { return lds_frag(xrd[st & 1] + ((st >> 1) & 1) * 12288 + t * 16 * 128); };

  issue_w(0); issue_x(0); issue_w(1); issue_w(2); issue_x(1);
  wait_vm<10>();
  raw_barrier();
#pragma unroll
  for (int t = 0; t < 3; ++t) xf[t] = read_x(0, t);
#pragma unroll
  for (int dt = 0; dt < 8; ++dt) af[dt] = read_a(0, dt);

  auto step = [&](auto ic) {
    constexpr int s = decltype(ic)::value;
    STAMP(1 + s);
    if constexpr (s < 15) wait_vm<step_wait(s)>();
    raw_barrier();
    if constexpr (n_w(s) > 0) issue_w(s + 3);
    if constexpr (n_x(s) > 0) issue_x((s + 3) / 2);
#pragma unroll
    for (int i = first_piece(s); i < first_piece(s + 1); ++i) issue_piece(i);
    if constexpr (s == 0) {
      const int l = (wave - 3) * 64 + lane;
      const int xr = l < 288 ? l : 0;
      const unsigned o = wave < 3 ? (unsigned)((128 * (wave + 1) + 8 * b + (lane >> 3)) * (HD * 2) + (lane & 7) * 128)
                                  : (unsigned)(((idx & 1) * 48 + xr / 6) * (DM * 2) + (2 + xr % 6) * 128);
      touch(o, wave < 3 ? wq : x);
    }
    if constexpr (s == 8) touch((unsigned)(b * 32768 + wave * 4096 + (lane & 31) * 128), wo);
    bf16x8 xn[3], an[8];
    if constexpr (s < 15) {
#pragma unroll
      for (int t = 0; t < 3; ++t) xn[t] = read_x(s + 1, t);
      an[0] = read_a(s + 1, 0);
      an[1] = read_a(s + 1, 1);
    }
    __builtin_amdgcn_sched_barrier(0);
#pragma unroll
    for (int dt = 0; dt < 8; ++dt) {
#pragma unroll
      for (int t = 0; t < 3; ++t) {
        if constexpr (s == 0) mfma_init(qacc[dt][t], af[dt], xf[t]);
        else mfma_acc(qacc[dt][t], af[dt], xf[t]);
      }
      if constexpr (s < 15) {
        if (dt + 2 < 8) an[dt + 2] = read_a(s + 1, dt + 2);
      }
      __builtin_amdgcn_sched_barrier(0);
    }
    if constexpr (s < 15) {
#pragma unroll
      for (int t = 0; t < 3; ++t) xf[t] = xn[t];
#pragma unroll
      for (int dt = 0; dt < 8; ++dt) af[dt] = an[dt];
    }
  };
  step(IC<0>{}); step(IC<1>{}); step(IC<2>{}); step(IC<3>{}); step(IC<4>{}); step(IC<5>{}); step(IC<6>{}); step(IC<7>{});
  step(IC<8>{}); step(IC<9>{}); step(IC<10>{}); step(IC<11>{}); step(IC<12>{}); step(IC<13>{}); step(IC<14>{}); step(IC<15>{});

  asm volatile("s_nop 7\n\ts_nop 7" ::: "memory");
  __builtin_amdgcn_sched_barrier(0);
  STAMP(17);
  bf16x8 qf[4][3];
#pragma unroll
  for (int s = 0; s < 4; ++s)
#pragma unroll
    for (int t = 0; t < 3; ++t) {
      u32x4 r;
#pragma unroll
      for (int e = 0; e < 2; ++e) {
        const unsigned a = pack2(qacc[2 * s][t][2 * e], qacc[2 * s][t][2 * e + 1]);
        const unsigned bq = pack2(qacc[2 * s + 1][t][2 * e], qacc[2 * s + 1][t][2 * e + 1]);
        auto r1 = __builtin_amdgcn_permlane32_swap(a, bq, false, false);
        auto r2 = __builtin_amdgcn_permlane16_swap(r1[0], r1[1], false, false);
        r[e] = r2[0];
        r[2 + e] = r2[1];
      }
      qf[s][t] = __builtin_bit_cast(bf16x8, r);
    }

  wait_vm<cnt(15) - pos_p(11)>();
  raw_barrier();
  STAMP(18);

  f32x4 sacc[6][3];
#pragma unroll
  for (int kt = 0; kt < 6; ++kt)
#pragma unroll
    for (int t = 0; t < 3; ++t) sacc[kt][t] = (f32x4){0.f, 0.f, 0.f, 0.f};
#pragma unroll
  for (int pc = 0; pc < 3; ++pc) {
    const unsigned kblk = (blk_tab(0, pc) >> (8 * u)) & 255u;
    const unsigned kb = lds0 + kblk * 8192 + c * 256;
#pragma unroll
    for (int k2 = 0; k2 < 2; ++k2) {
#pragma unroll
      for (int s = 0; s < 4; ++s) {
        const bf16x8 kf = lds_frag(kb + k2 * 4096 + (((4 * s + g) ^ c) << 4));
#pragma unroll
        for (int t = 0; t < 3; ++t) sacc[pc * 2 + k2][t] = MFMA16(kf, qf[s][t], sacc[pc * 2 + k2][t]);
      }
    }
  }
  STAMP(19);
  raw_barrier();

  const unsigned ring = lds0 + (unsigned)((ring_tab() >> (8 * wave)) & 255u) * 8192;
  const int srow = lane >> 3, pos = lane & 7;
  const unsigned vo = (unsigned)(srow * (DM * 2) + (((((pos >> 1) ^ ((srow >> 1) & 3)) << 1) | (pos & 1)) << 4) + wave * 128);
  auto issue_wo = [&](int st, int e) {
    dma16(vo, wo + (size_t)(st * 32 + 8 * e) * (DM * 2), ring + (st & 1) * 4096 + e * 1024);
  };
#pragma unroll
  for (int i = 20; i < 24; ++i) issue_piece(i);
#pragma unroll
  for (int e = 0; e < 8; ++e) issue_wo(e >> 2, e & 3);

  const float cexp = 0.08838834764831845f * 1.4426950408889634f;
  bf16x8 pf[3][3];
  float rinv[3];
#pragma unroll
  for (int t = 0; t < 3; ++t) {
    float m = sacc[0][t][0];
#pragma unroll
    for (int kt = 0; kt < 6; ++kt)
#pragma unroll
      for (int r = 0; r < 4; ++r) m = fmaxf(m, sacc[kt][t][r]);
    m = xg_max(m);
    const float mc = m * cexp;
    float sum = 0.f;
#pragma unroll
    for (int kt = 0; kt < 6; ++kt)
#pragma unroll
      for (int r = 0; r < 4; ++r) {
        const float e = __builtin_amdgcn_exp2f(fmaf(sacc[kt][t][r], cexp, -mc));
        sacc[kt][t][r] = e;
        sum += e;
      }
    sum = xg_sum(sum);
    rinv[t] = 1.0f / sum;
#pragma unroll
    for (int s = 0; s < 3; ++s) {
      u32x4 r;
      r[0] = pack2(sacc[2 * s][t][0], sacc[2 * s][t][1]);
      r[1] = pack2(sacc[2 * s][t][2], sacc[2 * s][t][3]);
      r[2] = pack2(sacc[2 * s + 1][t][0], sacc[2 * s + 1][t][1]);
      r[3] = pack2(sacc[2 * s + 1][t][2], sacc[2 * s + 1][t][3]);
      pf[s][t] = __builtin_bit_cast(bf16x8, r);
    }
  }

  STAMP(20);
  f32x4 oacc[8][3];
#pragma unroll
  for (int dt = 0; dt < 8; ++dt)
#pragma unroll
    for (int t = 0; t < 3; ++t) oacc[dt][t] = (f32x4){0.f, 0.f, 0.f, 0.f};
  const unsigned vlane = lds0 + (4 * g + qp) * 256 + 8 * p;
#pragma unroll
  for (int s = 0; s < 3; ++s) {
    if (s == 0) { wait_vm<12>(); raw_barrier(); }
    if (s == 2) { wait_vm<8>(); raw_barrier(); }
    const unsigned vblk = (blk_tab(1, s) >> (8 * u)) & 255u;
    const unsigned vb = vlane + vblk * 8192;
#pragma unroll
    for (int dt = 0; dt < 8; ++dt) {
      const unsigned a0 = vb + ((dt ^ fl) << 5);
      const bf16x8 vf = cat8(tr_read(a0), tr_read(a0 + 16 * 256));
#pragma unroll
      for (int t = 0; t < 3; ++t) oacc[dt][t] = MFMA16(vf, pf[s][t], oacc[dt][t]);
    }
  }
  STAMP(21);
  raw_barrier();

#pragma unroll
  for (int s = 0; s < 4; ++s)
#pragma unroll
    for (int t = 0; t < 3; ++t) {
      const float ri = rinv[t];
      u32x4 r;
      r[0] = pack2(oacc[2 * s][t][0] * ri, oacc[2 * s][t][1] * ri);
      r[1] = pack2(oacc[2 * s][t][2] * ri, oacc[2 * s][t][3] * ri);
      r[2] = pack2(oacc[2 * s + 1][t][0] * ri, oacc[2 * s + 1][t][1] * ri);
      r[3] = pack2(oacc[2 * s + 1][t][2] * ri, oacc[2 * s + 1][t][3] * ri);
      const int f = s * 6 + qh * 3 + t;
      const unsigned tab = (f >> 3) == 0 ? blk_tab(1, 0) : (f >> 3) == 1 ? blk_tab(1, 1) : blk_tab(1, 2);
      const unsigned a = lds0 + ((tab >> (8 * u)) & 255u) * 8192 + (f & 7) * 1024 + lane * 16;
      *(lds_u32x4_ptr)(uintptr_t)a = r;
    }
  wait_vm<4>();
  raw_barrier();
  STAMP(22);

  const int fl2 = (2 * g + (qp >> 1)) & 3;
  unsigned wrd[4];
#pragma unroll
  for (int j = 0; j < 4; ++j) wrd[j] = ring + (4 * g + qp) * 128 + 8 * p + ((j ^ fl2) << 5);
  const unsigned olane = lds0 + lane * 16;
  auto read_wo = [&](int st, int j) {
    const unsigned a0 = wrd[j] + (st & 1) * 4096;
    return cat8(tr_read(a0), tr_read(a0 + 16 * 128));
  };
  auto read_o = [&](int st, int t) {
    const int f = (st & 3) * 6 + t;
    return lds_frag(olane + piece_blk(12 + (f >> 3) * 4 + (st >> 2)) * 8192 + (f & 7) * 1024);
  };
  f32x4 acc[4][6];
  bf16x8 wf[4], of[3];
#pragma unroll
  for (int j = 0; j < 4; ++j) wf[j] = read_wo(0, j);
  of[0] = read_o(0, 0);
  of[1] = read_o(0, 1);
  auto ostep = [&](auto ic) {
    constexpr int s = decltype(ic)::value;
    bf16x8 wn[4];
#pragma unroll
    for (int t = 0; t < 6; ++t) {
      if (s * 6 + t + 2 < 96) of[(t + 2) % 3] = read_o(s + (t + 2) / 6, (t + 2) % 6);
      if constexpr (s < 15) {
        if (t == 4) {
          if constexpr (s < 14) wait_vm<4>(); else wait_vm<0>();
#pragma unroll
          for (int j = 0; j < 4; ++j) wn[j] = read_wo(s + 1, j);
        }
      }
      __builtin_amdgcn_sched_barrier(0);
#pragma unroll
      for (int j = 0; j < 4; ++j) {
        if constexpr (s == 0) mfma_init(acc[j][t], wf[j], of[t % 3]);
        else mfma_acc(acc[j][t], wf[j], of[t % 3]);
      }
      if constexpr (s < 14) {
        if (t < 4) issue_wo(s + 2, t);
      }
      __builtin_amdgcn_sched_barrier(0);
    }
    if constexpr (s < 15) {
#pragma unroll
      for (int j = 0; j < 4; ++j) wf[j] = wn[j];
    }
  };
  ostep(IC<0>{}); ostep(IC<1>{}); ostep(IC<2>{}); ostep(IC<3>{}); ostep(IC<4>{}); ostep(IC<5>{}); ostep(IC<6>{}); ostep(IC<7>{});
  STAMP(23);
  ostep(IC<8>{}); ostep(IC<9>{}); ostep(IC<10>{}); ostep(IC<11>{}); ostep(IC<12>{}); ostep(IC<13>{}); ostep(IC<14>{}); ostep(IC<15>{});
  asm volatile("s_nop 7\n\ts_nop 7" ::: "memory");
  __builtin_amdgcn_sched_barrier(0);
  STAMP(24);

  u32x4* pw = p_ws + ((((size_t)(blk * NB + b) * 8 + hg) * 8 + wave) * 12) * 64 + lane;
#pragma unroll
  for (int t = 0; t < 6; ++t)
#pragma unroll
    for (int jp = 0; jp < 2; ++jp) {
      u32x4 r;
      r[0] = pack2(acc[2 * jp][t][0], acc[2 * jp][t][1]);
      r[1] = pack2(acc[2 * jp][t][2], acc[2 * jp][t][3]);
      r[2] = pack2(acc[2 * jp + 1][t][0], acc[2 * jp + 1][t][1]);
      r[3] = pack2(acc[2 * jp + 1][t][2], acc[2 * jp + 1][t][3]);
      pw[(t * 2 + jp) * 64] = r;
    }
  STAMP(25);
}

__global__ __launch_bounds__(768) void reduce_kernel(const u32x4* __restrict__ p_ws, unsigned short* __restrict__ out) {
  const int tid = threadIdx.x;
  const int lane = tid & 63, i = tid >> 6;
  const int bid = blockIdx.x;
  const int w = bid & 7, bb = bid >> 3;
  const u32x4* src = p_ws + (((size_t)bb * 8) * 8 + w) * 12 * 64 + tid;
  u32x4 v[8];
#pragma unroll
  for (int hg = 0; hg < 8; ++hg) v[hg] = src[(size_t)hg * 8 * 12 * 64];
  float s[8];
#pragma unroll
  for (int e = 0; e < 4; ++e) {
    float lo = 0.f, hi = 0.f;
#pragma unroll
    for (int hg = 0; hg < 8; ++hg) {
      lo += __uint_as_float(v[hg][e] << 16);
      hi += __uint_as_float(v[hg][e] & 0xffff0000u);
    }
    s[2 * e] = lo;
    s[2 * e + 1] = hi;
  }
  const int c = lane & 15, g = lane >> 4;
  const int q = (i >> 1) * 16 + c;
  const int n = w * 64 + (i & 1) * 32 + 4 * g;
  unsigned short* o = out + (size_t)((bb & 15) * SQ + q) * 1024 + (bb >> 4) * 512 + n;
  u32x2 r0, r1;
  r0[0] = pack2(s[0], s[1]); r0[1] = pack2(s[2], s[3]);
  r1[0] = pack2(s[4], s[5]); r1[1] = pack2(s[6], s[7]);
  *(u32x2*)o = r0;
  *(u32x2*)(o + 16) = r1;
}

}

extern "C" void kernel_launch(void* const* d_in, const int* in_sizes, int n_in, void* d_out, int out_size,
                              void* d_ws, size_t ws_size, hipStream_t stream) {
  const short* xA = (const short*)d_in[0];
  const short* kA = (const short*)d_in[1];
  const short* vA = (const short*)d_in[2];
  const short* wqA = (const short*)d_in[3];
  const short* woA = (const short*)d_in[4];
  const short* xB = (const short*)d_in[5];
  const short* kB = (const short*)d_in[6];
  const short* vB = (const short*)d_in[7];
  const short* wqB = (const short*)d_in[8];
  const short* woB = (const short*)d_in[9];
  u32x4* p_ws = (u32x4*)d_ws;
  (void)hipFuncSetAttribute((const void*)fused_kernel, hipFuncAttributeMaxDynamicSharedMemorySize, K1_LDS);
  hipLaunchKernelGGL(fused_kernel, dim3(256), dim3(512), K1_LDS, stream, xA, kA, vA, wqA, woA, xB, kB, vB, wqB,
                     woB, p_ws);
  hipLaunchKernelGGL(reduce_kernel, dim3(256), dim3(768), 0, stream, (const u32x4*)p_ws, (unsigned short*)d_out);
}
```

```cpp
#include <hip/hip_runtime.h>
#include <hip/hip_bf16.h>
#include <stdint.h>

typedef __attribute__((ext_vector_type(8))) short bf16x8;
typedef __attribute__((ext_vector_type(4))) short s16x4;
typedef __attribute__((ext_vector_type(4))) float f32x4;
typedef __attribute__((ext_vector_type(2))) float f32x2;
typedef __attribute__((ext_vector_type(4))) unsigned u32x4;
typedef __attribute__((ext_vector_type(2))) unsigned u32x2;
typedef __attribute__((ext_vector_type(2))) __bf16 bf16x2_t;
typedef __attribute__((address_space(3))) s16x4* lds_s16x4_ptr;

#define DI __device__ __forceinline__
#define MFMA16(a, b, c) __builtin_amdgcn_mfma_f32_16x16x32_bf16((a), (b), (c), 0, 0, 0)

#define STAMP(i)
namespace {

constexpr int NB = 16, SQ = 96, DM = 512, HQ = 32, DH = 128, HD = HQ * DH;

DI unsigned pack2(float a, float b) {
  f32x2 v = {a, b};
  return __builtin_bit_cast(unsigned, __builtin_convertvector(v, bf16x2_t));
}
DI s16x4 tr_read(unsigned lds_addr) {
  return __builtin_amdgcn_ds_read_tr16_b64_v4i16((lds_s16x4_ptr)(uintptr_t)lds_addr);
}
DI bf16x8 cat8(s16x4 lo, s16x4 hi) { return __builtin_shufflevector(lo, hi, 0, 1, 2, 3, 4, 5, 6, 7); }
DI float xg_max(float x) {
  auto s = __builtin_amdgcn_permlane16_swap(__float_as_uint(x), __float_as_uint(x), false, false);
  x = fmaxf(__uint_as_float(s[0]), __uint_as_float(s[1]));
  auto t = __builtin_amdgcn_permlane32_swap(__float_as_uint(x), __float_as_uint(x), false, false);
  return fmaxf(__uint_as_float(t[0]), __uint_as_float(t[1]));
}
DI float xg_sum(float x) {
  auto s = __builtin_amdgcn_permlane16_swap(__float_as_uint(x), __float_as_uint(x), false, false);
  x = __uint_as_float(s[0]) + __uint_as_float(s[1]);
  auto t = __builtin_amdgcn_permlane32_swap(__float_as_uint(x), __float_as_uint(x), false, false);
  return __uint_as_float(t[0]) + __uint_as_float(t[1]);
}

constexpr int K1_LDS = 160 * 1024;
constexpr int XBASE = 96 * 1024;

constexpr int piece_blk(int i) {
  constexpr int tb[24] = {15, 16, 17, 18, 19, 4, 5, 6, 7, 12, 8, 9,
                          10, 11, 0, 1, 2, 3, 13, 14, 15, 16, 17, 18};
  return tb[i];
}
constexpr unsigned blk_tab(int kind, int pc) {
  unsigned r = 0;
  for (int u = 0; u < 4; ++u) r |= (unsigned)piece_blk(kind * 12 + pc * 4 + u) << (8 * u);
  return r;
}
constexpr int ring_blk(int w) {
  return piece_blk(4 + w);
}
constexpr unsigned long long ring_tab() {
  unsigned long long r = 0;
  for (int w = 0; w < 8; ++w) r |= (unsigned long long)ring_blk(w) << (8 * w);
  return r;
}

constexpr int n_w(int s) { return (s + 3 <= 15) ? 4 : 0; }
constexpr int n_x(int s) { return ((s & 1) && ((s + 3) / 2 <= 7)) ? 2 : 0; }
constexpr int first_piece(int s) {
  return s <= 2 ? 0 : s <= 4 ? 2 : s <= 13 ? s - 1 : s == 14 ? 12 : s == 15 ? 14 : 20;
}
constexpr int n_p(int s) { return first_piece(s + 1) - first_piece(s); }
constexpr int n_t(int s) { return s <= 9 ? 1 : 0; }
constexpr int cnt(int s) {
  int n = 16;
  for (int j = 0; j <= s; ++j) n += n_w(j) + n_x(j) + n_p(j) + n_t(j);
  return n;
}
constexpr int pos_w(int st) { return st == 0 ? 4 : st == 1 ? 10 : st == 2 ? 14 : cnt(st - 4) + 4; }
constexpr int pos_x(int j) { return j == 0 ? 6 : j == 1 ? 16 : cnt(2 * j - 4) + 6; }
constexpr int pos_p(int i) {
  int s = 0;
  while (first_piece(s + 1) <= i) ++s;
  return cnt(s - 1) + n_w(s) + n_x(s) + (i - first_piece(s)) + 1;
}
constexpr int cmax(int a, int b) { return a > b ? a : b; }
constexpr int step_wait(int s) {
  int need = s < 15 ? cmax(pos_w(s + 1), pos_x((s + 1) >> 1)) : 0;
  if (s == 13) need = cmax(need, pos_p(9));
  if (s == 14) need = cmax(need, pos_p(11));
  return cnt(s - 1) - need;
}

template <int N> DI void wait_vm() { asm volatile("s_waitcnt vmcnt(%0)" ::"n"(N) : "memory"); }
DI void raw_barrier() {
  asm volatile("s_waitcnt lgkmcnt(0)" ::: "memory");
  __builtin_amdgcn_s_barrier();
  asm volatile("" ::: "memory");
}
DI void dma16(unsigned voff, const void* sbase, unsigned lds_dst) {
  unsigned keep;
  asm volatile("s_mov_b32 %0, m0\n\ts_mov_b32 m0, %3\n\ts_nop 0\n\tglobal_load_lds_dwordx4 %1, %2\n\ts_mov_b32 m0, %0"
               : "=&s"(keep) : "v"(voff), "s"(sbase), "s"(lds_dst) : "memory");
}
DI void dma16_nt(unsigned voff, const void* sbase, unsigned lds_dst) {
  unsigned keep;
  asm volatile("s_mov_b32 %0, m0\n\ts_mov_b32 m0, %3\n\ts_nop 0\n\tglobal_load_lds_dwordx4 %1, %2 nt\n\ts_mov_b32 m0, %0"
               : "=&s"(keep) : "v"(voff), "s"(sbase), "s"(lds_dst) : "memory");
}
template <int MASK> DI void touch(const char* a) {
  asm volatile("s_mov_b64 exec, %1\n\tglobal_load_dword v255, %0, off\n\ts_mov_b64 exec, -1" ::"v"(a), "n"(MASK) : "v255", "memory");
}
DI void dma16_48(unsigned voff, const void* sbase, unsigned lds_dst) {
  unsigned keep;
  asm volatile("s_mov_b32 %0, m0\n\ts_mov_b32 m0, %3\n\ts_mov_b32 exec_hi, 0xffff\n\tglobal_load_lds_dwordx4 %1, %2\n\ts_mov_b32 exec_hi, -1\n\ts_mov_b32 m0, %0"
               : "=&s"(keep) : "v"(voff), "s"(sbase), "s"(lds_dst) : "memory");
}
DI void gload_nt(u32x4& d, unsigned voff, const void* sbase) {
  asm volatile("global_load_dwordx4 %0, %1, %2 nt" : "=v"(d) : "v"(voff), "s"(sbase) : "memory");
}
DI void mfma_acc(f32x4& c, bf16x8 a, bf16x8 b) {
  asm volatile("v_mfma_f32_16x16x32_bf16 %0, %1, %2, %0" : "+v"(c) : "v"(a), "v"(b));
}
DI void mfma_init(f32x4& c, bf16x8 a, bf16x8 b) {
  asm volatile("v_mfma_f32_16x16x32_bf16 %0, %1, %2, 0" : "=&v"(c) : "v"(a), "v"(b));
}
template <int V> struct IC { static constexpr int value = V; };
typedef const __attribute__((address_space(3))) bf16x8* lds_frag_ptr;
typedef __attribute__((address_space(3))) u32x4* lds_u32x4_ptr;
DI bf16x8 lds_frag(unsigned a) { return *(lds_frag_ptr)(uintptr_t)a; }

__global__ __launch_bounds__(512, 2) void fused_kernel(
    const short* __restrict__ xA, const short* __restrict__ kA, const short* __restrict__ vA,
    const short* __restrict__ wqA, const short* __restrict__ woA, const short* __restrict__ xB,
    const short* __restrict__ kB, const short* __restrict__ vB, const short* __restrict__ wqB,
    const short* __restrict__ woB, u32x4* __restrict__ p_ws) {
  extern __shared__ __attribute__((aligned(16))) char smem[];
  const int tid = threadIdx.x;
  const int lane = tid & 63;
  const int wave = __builtin_amdgcn_readfirstlane(tid >> 6);
  const int c = lane & 15, g = lane >> 4, qp = (lane & 15) >> 2, p = lane & 3;
  const int bid = blockIdx.x;
  const int xcd = bid & 7, idx = bid >> 3;
  const int blk = xcd >> 2;
  const int hg = (xcd & 3) * 2 + (idx & 1);
  const int b = idx >> 1;
  const int h0 = hg * 4;
  const int u = wave >> 1, qh = wave & 1;

  const char* __restrict__ x = (const char*)(blk ? xB : xA) + (size_t)b * SQ * DM * 2;
  const char* __restrict__ kc = (const char*)(blk ? kB : kA) + ((size_t)b * HQ + h0) * (SQ * DH * 2);
  const char* __restrict__ vc = (const char*)(blk ? vB : vA) + ((size_t)b * HQ + h0) * (SQ * DH * 2);
  const char* __restrict__ wq = (const char*)(blk ? wqB : wqA) + h0 * DH * 2;
  const char* __restrict__ wo = (const char*)(blk ? woB : woA) + (size_t)h0 * DH * DM * 2;

  const unsigned lds0 = (unsigned)(uintptr_t)smem;
  STAMP(0);

  unsigned vw[2], vx[2], vk, vv;
#pragma unroll
  for (int e = 0; e < 2; ++e) {
    const int f = g | (e << 2);
    const int ch = (((c >> 1) ^ f) << 1) | (c & 1);
    vw[e] = (unsigned)((16 * qh + g) * (HD * 2) + u * (DH * 2) + ch * 16);
    const int r = 12 * wave + 6 * e + (lane >> 3);
    vx[e] = (unsigned)(r * (DM * 2) + (((lane & 7) ^ ((r >> 1) & 7)) << 4));
  }
  {
    const int row = 4 * wave + g;
    vk = (unsigned)(row * 256 + ((c ^ (row & 15)) << 4));
    vv = (unsigned)(row * 256 + (((((c >> 1) ^ (row & 7)) << 1) | (c & 1)) << 4));
  }

  auto issue_w = [&](int st, int e) {
    dma16(vw[e >> 1], wq + (size_t)(st * 32 + 4 * e) * (HD * 2), lds0 + (st % 3) * 32768 + (wave * 4 + e) * 1024);
  };
  auto issue_x = [&](int j, int e) {
    dma16_48(vx[e], x + j * 128, lds0 + XBASE + (j & 1) * 12288 + (12 * wave + 6 * e) * 128);
  };
  u32x4 kst[7];
  auto piece_src = [&](int i) { return (i >= 12 ? vc : kc) + (i & 3) * (SQ * DH * 2) + ((i % 12) >> 2) * 8192; };
  auto piece_dst = [&](int i) { return lds0 + piece_blk(i) * 8192 + wave * 1024; };
  auto issue_piece = [&](auto ic) {
    constexpr int i = decltype(ic)::value;
    if constexpr (i >= 5 && i < 12) gload_nt(kst[i - 5], vk, piece_src(i));
    else dma16_nt(i >= 12 ? vv : vk, piece_src(i), piece_dst(i));
  };
  const int tl = wave * 10 + lane;
  const char* ta = tl < 32 ? wq + (size_t)((4 + (tl >> 4)) * 32 + 2 * b + ((tl >> 3) & 1)) * (HD * 2) + (tl & 7) * 128
                           : x + ((idx & 1) * 48 + (tl - 32)) * (DM * 2) + 3 * 128;
  const unsigned tinc = tl < 32 ? 64u * (HD * 2) : 128u;

  const int fl = qp | ((g & 1) << 2);
  unsigned trw[8];
#pragma unroll
  for (int dt = 0; dt < 8; ++dt) trw[dt] = lds0 + u * 8192 + (8 * g + qp) * 256 + 8 * p + ((dt ^ fl) << 5);
  unsigned xrd[2];
#pragma unroll
  for (int hf = 0; hf < 2; ++hf)
    xrd[hf] = lds0 + XBASE + (qh * 48 + c) * 128 + (((4 * hf + g) ^ ((c >> 1) & 7)) << 4);

  f32x4 qacc[8][3];
  bf16x8 af[8], xf[3];
  auto read_a = [&](int st, int dt) {
    const unsigned a0 = trw[dt] + (st % 3) * 32768;
    return cat8(tr_read(a0), tr_read(a0 + 4 * 256));
  };
  auto read_x = [&](int st, int t) { return lds_frag(xrd[st & 1] + ((st >> 1) & 1) * 12288 + t * 16 * 128); };

#pragma unroll
  for (int e = 0; e < 4; ++e) issue_w(0, e);
  issue_x(0, 0); issue_x(0, 1);
#pragma unroll
  for (int e = 0; e < 8; ++e) issue_w(1 + (e >> 2), e & 3);
  issue_x(1, 0); issue_x(1, 1);
  wait_vm<10>();
  raw_barrier();
#pragma unroll
  for (int t = 0; t < 3; ++t) xf[t] = read_x(0, t);
#pragma unroll
  for (int dt = 0; dt < 8; ++dt) af[dt] = read_a(0, dt);

  auto mem_op = [&](auto sc, auto kc_) {
    constexpr int s = decltype(sc)::value, k = decltype(kc_)::value;
    if constexpr (k < n_w(s)) issue_w(s + 3, k);
    else if constexpr (k < n_w(s) + n_x(s)) issue_x((s + 3) / 2, k - n_w(s));
    else if constexpr (k < n_w(s) + n_x(s) + n_p(s)) issue_piece(IC<first_piece(s) + k - n_w(s) - n_x(s)>{});
    else if constexpr (k < n_w(s) + n_x(s) + n_p(s) + n_t(s)) {
      if constexpr (s <= 5) {
        touch<0x3ff>(ta);
        ta += (s == 4 && tl >= 32) ? 0u : tinc;
      } else {
        touch<0xff>(wo + b * 32768 + ((s - 6) * 64 + wave * 8 + (lane & 7)) * 128);
      }
    } else if constexpr (s == 13 && k < 5) {
      *(lds_u32x4_ptr)(uintptr_t)(piece_dst(5 + k) + lane * 16) = kst[k];
    } else if constexpr (s == 14 && k < 4) {
      *(lds_u32x4_ptr)(uintptr_t)(piece_dst(8 + k) + lane * 16) = kst[3 + k];
    }
  };
  auto group = [&](auto sc, auto dc, bf16x8* an) {
    constexpr int s = decltype(sc)::value, dt = decltype(dc)::value;
#pragma unroll
    for (int t = 0; t < 3; ++t) {
      if constexpr (s == 0) mfma_init(qacc[dt][t], af[dt], xf[t]);
      else mfma_acc(qacc[dt][t], af[dt], xf[t]);
    }
    if constexpr (s < 15 && dt + 2 < 8) an[dt + 2] = read_a(s + 1, dt + 2);
    mem_op(sc, dc);
    __builtin_amdgcn_sched_barrier(0);
  };
  auto step = [&](auto ic) {
    constexpr int s = decltype(ic)::value;
    STAMP(1 + s);
    if constexpr (s < 15) wait_vm<step_wait(s)>();
    raw_barrier();
    bf16x8 xn[3], an[8];
    if constexpr (s < 15) {
#pragma unroll
      for (int t = 0; t < 3; ++t) xn[t] = read_x(s + 1, t);
      an[0] = read_a(s + 1, 0);
      an[1] = read_a(s + 1, 1);
    }
    __builtin_amdgcn_sched_barrier(0);
    group(ic, IC<0>{}, an); group(ic, IC<1>{}, an); group(ic, IC<2>{}, an); group(ic, IC<3>{}, an);
    group(ic, IC<4>{}, an); group(ic, IC<5>{}, an); group(ic, IC<6>{}, an); group(ic, IC<7>{}, an);
    if constexpr (s < 15) {
#pragma unroll
      for (int t = 0; t < 3; ++t) xf[t] = xn[t];
#pragma unroll
      for (int dt = 0; dt < 8; ++dt) af[dt] = an[dt];
    }
  };
  step(IC<0>{}); step(IC<1>{}); step(IC<2>{}); step(IC<3>{}); step(IC<4>{}); step(IC<5>{}); step(IC<6>{}); step(IC<7>{});
  step(IC<8>{}); step(IC<9>{}); step(IC<10>{}); step(IC<11>{}); step(IC<12>{}); step(IC<13>{}); step(IC<14>{}); step(IC<15>{});

  asm volatile("s_nop 7\n\ts_nop 7" ::: "memory");
  __builtin_amdgcn_sched_barrier(0);
  STAMP(17);
  u32x4 vst[4];
#pragma unroll
  for (int i = 0; i < 4; ++i) gload_nt(vst[i], vv, piece_src(20 + i));
  bf16x8 qf[4][3];
#pragma unroll
  for (int s = 0; s < 4; ++s)
#pragma unroll
    for (int t = 0; t < 3; ++t) {
      u32x4 r;
#pragma unroll
      for (int e = 0; e < 2; ++e) {
        const unsigned a = pack2(qacc[2 * s][t][2 * e], qacc[2 * s][t][2 * e + 1]);
        const unsigned bq = pack2(qacc[2 * s + 1][t][2 * e], qacc[2 * s + 1][t][2 * e + 1]);
        auto r1 = __builtin_amdgcn_permlane32_swap(a, bq, false, false);
        auto r2 = __builtin_amdgcn_permlane16_swap(r1[0], r1[1], false, false);
        r[e] = r2[0];
        r[2 + e] = r2[1];
      }
      qf[s][t] = __builtin_bit_cast(bf16x8, r);
    }

  raw_barrier();
  STAMP(18);

  f32x4 sacc[6][3];
#pragma unroll
  for (int kt = 0; kt < 6; ++kt)
#pragma unroll
    for (int t = 0; t < 3; ++t) sacc[kt][t] = (f32x4){0.f, 0.f, 0.f, 0.f};
#pragma unroll
  for (int pc = 0; pc < 3; ++pc) {
    const unsigned kblk = (blk_tab(0, pc) >> (8 * u)) & 255u;
    const unsigned kb = lds0 + kblk * 8192 + c * 256;
#pragma unroll
    for (int k2 = 0; k2 < 2; ++k2) {
#pragma unroll
      for (int s = 0; s < 4; ++s) {
        const bf16x8 kf = lds_frag(kb + k2 * 4096 + (((4 * s + g) ^ c) << 4));
#pragma unroll
        for (int t = 0; t < 3; ++t) sacc[pc * 2 + k2][t] = MFMA16(kf, qf[s][t], sacc[pc * 2 + k2][t]);
      }
    }
  }
  STAMP(19);
  raw_barrier();

  const unsigned ring = lds0 + (unsigned)((ring_tab() >> (8 * wave)) & 255u) * 8192;
  const int srow = lane >> 3, pos = lane & 7;
  const unsigned vo = (unsigned)(srow * (DM * 2) + (((((pos >> 1) ^ ((srow >> 1) & 3)) << 1) | (pos & 1)) << 4) + wave * 128);
  auto issue_wo = [&](int st, int e) {
    dma16(vo, wo + (size_t)(st * 32 + 8 * e) * (DM * 2), ring + (st & 1) * 4096 + e * 1024);
  };
#pragma unroll
  for (int e = 0; e < 8; ++e) issue_wo(e >> 2, e & 3);

  const float cexp = 0.08838834764831845f * 1.4426950408889634f;
  bf16x8 pf[3][3];
  float rinv[3];
#pragma unroll
  for (int t = 0; t < 3; ++t) {
    float m = sacc[0][t][0];
#pragma unroll
    for (int kt = 0; kt < 6; ++kt)
#pragma unroll
      for (int r = 0; r < 4; ++r) m = fmaxf(m, sacc[kt][t][r]);
    m = xg_max(m);
    const float mc = m * cexp;
    float sum = 0.f;
#pragma unroll
    for (int kt = 0; kt < 6; ++kt)
#pragma unroll
      for (int r = 0; r < 4; ++r) {
        const float e = __builtin_amdgcn_exp2f(fmaf(sacc[kt][t][r], cexp, -mc));
        sacc[kt][t][r] = e;
        sum += e;
      }
    sum = xg_sum(sum);
    rinv[t] = 1.0f / sum;
#pragma unroll
    for (int s = 0; s < 3; ++s) {
      u32x4 r;
      r[0] = pack2(sacc[2 * s][t][0], sacc[2 * s][t][1]);
      r[1] = pack2(sacc[2 * s][t][2], sacc[2 * s][t][3]);
      r[2] = pack2(sacc[2 * s + 1][t][0], sacc[2 * s + 1][t][1]);
      r[3] = pack2(sacc[2 * s + 1][t][2], sacc[2 * s + 1][t][3]);
      pf[s][t] = __builtin_bit_cast(bf16x8, r);
    }
  }

  STAMP(20);
  f32x4 oacc[8][3];
#pragma unroll
  for (int dt = 0; dt < 8; ++dt)
#pragma unroll
    for (int t = 0; t < 3; ++t) oacc[dt][t] = (f32x4){0.f, 0.f, 0.f, 0.f};
  const unsigned vlane = lds0 + (4 * g + qp) * 256 + 8 * p;
  wait_vm<8>();
#pragma unroll
  for (int i = 0; i < 4; ++i) *(lds_u32x4_ptr)(uintptr_t)(piece_dst(20 + i) + lane * 16) = vst[i];
  raw_barrier();
#pragma unroll
  for (int s = 0; s < 3; ++s) {
    const unsigned vblk = (blk_tab(1, s) >> (8 * u)) & 255u;
    const unsigned vb = vlane + vblk * 8192;
#pragma unroll
    for (int dt = 0; dt < 8; ++dt) {
      const unsigned a0 = vb + ((dt ^ fl) << 5);
      const bf16x8 vf = cat8(tr_read(a0), tr_read(a0 + 16 * 256));
#pragma unroll
      for (int t = 0; t < 3; ++t) oacc[dt][t] = MFMA16(vf, pf[s][t], oacc[dt][t]);
    }
  }
  STAMP(21);
  raw_barrier();

#pragma unroll
  for (int s = 0; s < 4; ++s)
#pragma unroll
    for (int t = 0; t < 3; ++t) {
      const float ri = rinv[t];
      u32x4 r;
      r[0] = pack2(oacc[2 * s][t][0] * ri, oacc[2 * s][t][1] * ri);
      r[1] = pack2(oacc[2 * s][t][2] * ri, oacc[2 * s][t][3] * ri);
      r[2] = pack2(oacc[2 * s + 1][t][0] * ri, oacc[2 * s + 1][t][1] * ri);
      r[3] = pack2(oacc[2 * s + 1][t][2] * ri, oacc[2 * s + 1][t][3] * ri);
      const int f = s * 6 + qh * 3 + t;
      const unsigned tab = (f >> 3) == 0 ? blk_tab(1, 0) : (f >> 3) == 1 ? blk_tab(1, 1) : blk_tab(1, 2);
      const unsigned a = lds0 + ((tab >> (8 * u)) & 255u) * 8192 + (f & 7) * 1024 + lane * 16;
      *(lds_u32x4_ptr)(uintptr_t)a = r;
    }
  wait_vm<4>();
  raw_barrier();
  STAMP(22);

  const int fl2 = (2 * g + (qp >> 1)) & 3;
  unsigned wrd[4];
#pragma unroll
  for (int j = 0; j < 4; ++j) wrd[j] = ring + (4 * g + qp) * 128 + 8 * p + ((j ^ fl2) << 5);
  const unsigned olane = lds0 + lane * 16;
  auto read_wo = [&](int st, int j) {
    const unsigned a0 = wrd[j] + (st & 1) * 4096;
    return cat8(tr_read(a0), tr_read(a0 + 16 * 128));
  };
  auto read_o = [&](int st, int t) {
    const int f = (st & 3) * 6 + t;
    return lds_frag(olane + piece_blk(12 + (f >> 3) * 4 + (st >> 2)) * 8192 + (f & 7) * 1024);
  };
  f32x4 acc[4][6];
  bf16x8 wf[4], of[3];
#pragma unroll
  for (int j = 0; j < 4; ++j) wf[j] = read_wo(0, j);
  of[0] = read_o(0, 0);
  of[1] = read_o(0, 1);
  auto ostep = [&](auto ic) {
    constexpr int s = decltype(ic)::value;
    bf16x8 wn[4];
#pragma unroll
    for (int t = 0; t < 6; ++t) {
      if (s * 6 + t + 2 < 96) of[(t + 2) % 3] = read_o(s + (t + 2) / 6, (t + 2) % 6);
      if constexpr (s < 15) {
        if (t == 4) {
          if constexpr (s < 14) wait_vm<4>(); else wait_vm<0>();
#pragma unroll
          for (int j = 0; j < 4; ++j) wn[j] = read_wo(s + 1, j);
        }
      }
      __builtin_amdgcn_sched_barrier(0);
#pragma unroll
      for (int j = 0; j < 4; ++j) {
        if constexpr (s == 0) mfma_init(acc[j][t], wf[j], of[t % 3]);
        else mfma_acc(acc[j][t], wf[j], of[t % 3]);
      }
      if constexpr (s < 14) {
        if (t < 4) issue_wo(s + 2, t);
      }
      __builtin_amdgcn_sched_barrier(0);
    }
    if constexpr (s < 15) {
#pragma unroll
      for (int j = 0; j < 4; ++j) wf[j] = wn[j];
    }
  };
  ostep(IC<0>{}); ostep(IC<1>{}); ostep(IC<2>{}); ostep(IC<3>{}); ostep(IC<4>{}); ostep(IC<5>{}); ostep(IC<6>{}); ostep(IC<7>{});
  STAMP(23);
  ostep(IC<8>{}); ostep(IC<9>{}); ostep(IC<10>{}); ostep(IC<11>{}); ostep(IC<12>{}); ostep(IC<13>{}); ostep(IC<14>{}); ostep(IC<15>{});
  asm volatile("s_nop 7\n\ts_nop 7" ::: "memory");
  __builtin_amdgcn_sched_barrier(0);
  STAMP(24);

  u32x4* pw = p_ws + ((((size_t)(blk * NB + b) * 8 + hg) * 8 + wave) * 12) * 64 + lane;
#pragma unroll
  for (int t = 0; t < 6; ++t)
#pragma unroll
    for (int jp = 0; jp < 2; ++jp) {
      u32x4 r;
      r[0] = pack2(acc[2 * jp][t][0], acc[2 * jp][t][1]);
      r[1] = pack2(acc[2 * jp][t][2], acc[2 * jp][t][3]);
      r[2] = pack2(acc[2 * jp + 1][t][0], acc[2 * jp + 1][t][1]);
      r[3] = pack2(acc[2 * jp + 1][t][2], acc[2 * jp + 1][t][3]);
      pw[(t * 2 + jp) * 64] = r;
    }
  STAMP(25);
}

__global__ __launch_bounds__(768) void reduce_kernel(const u32x4* __restrict__ p_ws, unsigned short* __restrict__ out) {
  const int tid = threadIdx.x;
  const int lane = tid & 63, i = tid >> 6;
  const int bid = blockIdx.x;
  const int w = bid & 7, bb = bid >> 3;
  const u32x4* src = p_ws + (((size_t)bb * 8) * 8 + w) * 12 * 64 + tid;
  u32x4 v[8];
#pragma unroll
  for (int hg = 0; hg < 8; ++hg) v[hg] = src[(size_t)hg * 8 * 12 * 64];
  float s[8];
#pragma unroll
  for (int e = 0; e < 4; ++e) {
    float lo = 0.f, hi = 0.f;
#pragma unroll
    for (int hg = 0; hg < 8; ++hg) {
      lo += __uint_as_float(v[hg][e] << 16);
      hi += __uint_as_float(v[hg][e] & 0xffff0000u);
    }
    s[2 * e] = lo;
    s[2 * e + 1] = hi;
  }
  const int c = lane & 15, g = lane >> 4;
  const int q = (i >> 1) * 16 + c;
  const int n = w * 64 + (i & 1) * 32 + 4 * g;
  unsigned short* o = out + (size_t)((bb & 15) * SQ + q) * 1024 + (bb >> 4) * 512 + n;
  u32x2 r0, r1;
  r0[0] = pack2(s[0], s[1]); r0[1] = pack2(s[2], s[3]);
  r1[0] = pack2(s[4], s[5]); r1[1] = pack2(s[6], s[7]);
  *(u32x2*)o = r0;
  *(u32x2*)(o + 16) = r1;
}

}

extern "C" void kernel_launch(void* const* d_in, const int* in_sizes, int n_in, void* d_out, int out_size,
                              void* d_ws, size_t ws_size, hipStream_t stream) {
  const short* xA = (const short*)d_in[0];
  const short* kA = (const short*)d_in[1];
  const short* vA = (const short*)d_in[2];
  const short* wqA = (const short*)d_in[3];
  const short* woA = (const short*)d_in[4];
  const short* xB = (const short*)d_in[5];
  const short* kB = (const short*)d_in[6];
  const short* vB = (const short*)d_in[7];
  const short* wqB = (const short*)d_in[8];
  const short* woB = (const short*)d_in[9];
  u32x4* p_ws = (u32x4*)d_ws;
  (void)hipFuncSetAttribute((const void*)fused_kernel, hipFuncAttributeMaxDynamicSharedMemorySize, K1_LDS);
  hipLaunchKernelGGL(fused_kernel, dim3(256), dim3(512), K1_LDS, stream, xA, kA, vA, wqA, woA, xB, kB, vB, wqB,
                     woB, p_ws);
  hipLaunchKernelGGL(reduce_kernel, dim3(256), dim3(768), 0, stream, (const u32x4*)p_ws, (unsigned short*)d_out);
}
```

```cpp
#include <hip/hip_runtime.h>
#include <hip/hip_bf16.h>
#include <stdint.h>

typedef __attribute__((ext_vector_type(8))) short bf16x8;
typedef __attribute__((ext_vector_type(4))) short s16x4;
typedef __attribute__((ext_vector_type(4))) float f32x4;
typedef __attribute__((ext_vector_type(2))) float f32x2;
typedef __attribute__((ext_vector_type(4))) unsigned u32x4;
typedef __attribute__((ext_vector_type(2))) unsigned u32x2;
typedef __attribute__((ext_vector_type(2))) __bf16 bf16x2_t;
typedef __attribute__((address_space(3))) s16x4* lds_s16x4_ptr;

#define DI __device__ __forceinline__
#define MFMA16(a, b, c) __builtin_amdgcn_mfma_f32_16x16x32_bf16((a), (b), (c), 0, 0, 0)

#define STAMP(i)
#define STAMPW(i)
#define STAMPEND(i)
namespace {

constexpr int NB = 16, SQ = 96, DM = 512, HQ = 32, DH = 128, HD = HQ * DH;

DI unsigned pack2(float a, float b) {
  f32x2 v = {a, b};
  return __builtin_bit_cast(unsigned, __builtin_convertvector(v, bf16x2_t));
}
DI s16x4 tr_read(unsigned lds_addr) {
  return __builtin_amdgcn_ds_read_tr16_b64_v4i16((lds_s16x4_ptr)(uintptr_t)lds_addr);
}
DI bf16x8 cat8(s16x4 lo, s16x4 hi) { return __builtin_shufflevector(lo, hi, 0, 1, 2, 3, 4, 5, 6, 7); }
DI float xg_max(float x) {
  auto s = __builtin_amdgcn_permlane16_swap(__float_as_uint(x), __float_as_uint(x), false, false);
  x = fmaxf(__uint_as_float(s[0]), __uint_as_float(s[1]));
  auto t = __builtin_amdgcn_permlane32_swap(__float_as_uint(x), __float_as_uint(x), false, false);
  return fmaxf(__uint_as_float(t[0]), __uint_as_float(t[1]));
}
DI float xg_sum(float x) {
  auto s = __builtin_amdgcn_permlane16_swap(__float_as_uint(x), __float_as_uint(x), false, false);
  x = __uint_as_float(s[0]) + __uint_as_float(s[1]);
  auto t = __builtin_amdgcn_permlane32_swap(__float_as_uint(x), __float_as_uint(x), false, false);
  return __uint_as_float(t[0]) + __uint_as_float(t[1]);
}

constexpr int K1_LDS = 160 * 1024;
constexpr int XBASE = 96 * 1024;

constexpr int piece_blk(int i) {
  constexpr int tb[24] = {15, 16, 17, 18, 19, 4, 5, 6, 7, 12, 8, 9,
                          10, 11, 0, 1, 2, 3, 13, 14, 15, 16, 17, 18};
  return tb[i];
}
constexpr unsigned blk_tab(int kind, int pc) {
  unsigned r = 0;
  for (int u = 0; u < 4; ++u) r |= (unsigned)piece_blk(kind * 12 + pc * 4 + u) << (8 * u);
  return r;
}
constexpr int ring_blk(int w) {
  return piece_blk(4 + w);
}
constexpr unsigned long long ring_tab() {
  unsigned long long r = 0;
  for (int w = 0; w < 8; ++w) r |= (unsigned long long)ring_blk(w) << (8 * w);
  return r;
}

constexpr int n_w(int s) { return (s + 3 <= 15) ? 4 : 0; }
constexpr int n_x(int s) { return ((s & 1) && ((s + 3) / 2 <= 7)) ? 2 : 0; }
constexpr int first_piece(int s) {
  return s <= 2 ? 0 : s <= 4 ? 2 : s <= 6 ? 4 : s <= 13 ? 5 : s == 14 ? 10 : s == 15 ? 14 : 20;
}
constexpr int n_p(int s) { return first_piece(s + 1) - first_piece(s); }
constexpr int n_t(int s) { return (s == 0 || s == 8) ? 1 : 0; }
constexpr int cnt(int s) {
  int n = 16;
  for (int j = 0; j <= s; ++j) n += n_w(j) + n_x(j) + n_p(j) + n_t(j);
  return n;
}
constexpr int pos_w(int st) { return st == 0 ? 4 : st == 1 ? 10 : st == 2 ? 14 : cnt(st - 4) + 4; }
constexpr int pos_x(int j) { return j == 0 ? 6 : j == 1 ? 16 : cnt(2 * j - 4) + 6; }
constexpr int pos_p(int i) {
  int s = 0;
  while (first_piece(s + 1) <= i) ++s;
  return cnt(s - 1) + n_w(s) + n_x(s) + (i - first_piece(s)) + 1;
}
constexpr int cmax(int a, int b) { return a > b ? a : b; }
constexpr int step_wait(int s) { return cnt(s - 1) - cmax(pos_w(s + 1), pos_x((s + 1) >> 1)); }

template <int N> DI void wait_vm() { asm volatile("s_waitcnt vmcnt(%0)" ::"n"(N) : "memory"); }
DI void raw_barrier() {
  asm volatile("s_waitcnt lgkmcnt(0)" ::: "memory");
  __builtin_amdgcn_s_barrier();
  asm volatile("" ::: "memory");
}
DI void dma16(unsigned voff, const void* sbase, unsigned lds_dst) {
  unsigned keep;
  asm volatile("s_mov_b32 %0, m0\n\ts_mov_b32 m0, %3\n\ts_nop 0\n\tglobal_load_lds_dwordx4 %1, %2\n\ts_mov_b32 m0, %0"
               : "=&s"(keep) : "v"(voff), "s"(sbase), "s"(lds_dst) : "memory");
}
DI void dma16_nt(unsigned voff, const void* sbase, unsigned lds_dst) {
  unsigned keep;
  asm volatile("s_mov_b32 %0, m0\n\ts_mov_b32 m0, %3\n\ts_nop 0\n\tglobal_load_lds_dwordx4 %1, %2 nt\n\ts_mov_b32 m0, %0"
               : "=&s"(keep) : "v"(voff), "s"(sbase), "s"(lds_dst) : "memory");
}
DI void touch(unsigned voff, const void* sbase) {
  asm volatile("global_load_dword v255, %0, %1" ::"v"(voff), "s"(sbase) : "v255", "memory");
}
DI void mfma_acc(f32x4& c, bf16x8 a, bf16x8 b) {
  asm volatile("v_mfma_f32_16x16x32_bf16 %0, %1, %2, %0" : "+v"(c) : "v"(a), "v"(b));
}
DI void mfma_init(f32x4& c, bf16x8 a, bf16x8 b) {
  asm volatile("v_mfma_f32_16x16x32_bf16 %0, %1, %2, 0" : "=&v"(c) : "v"(a), "v"(b));
}
template <int V> struct IC { static constexpr int value = V; };
typedef const __attribute__((address_space(3))) bf16x8* lds_frag_ptr;
typedef __attribute__((address_space(3))) u32x4* lds_u32x4_ptr;
DI bf16x8 lds_frag(unsigned a) { return *(lds_frag_ptr)(uintptr_t)a; }

__global__ __launch_bounds__(512, 2) void fused_kernel(
    const short* __restrict__ xA, const short* __restrict__ kA, const short* __restrict__ vA,
    const short* __restrict__ wqA, const short* __restrict__ woA, const short* __restrict__ xB,
    const short* __restrict__ kB, const short* __restrict__ vB, const short* __restrict__ wqB,
    const short* __restrict__ woB, u32x4* __restrict__ p_ws) {
  extern __shared__ __attribute__((aligned(16))) char smem[];
  const int tid = threadIdx.x;
  const int lane = tid & 63;
  const int wave = __builtin_amdgcn_readfirstlane(tid >> 6);
  const int c = lane & 15, g = lane >> 4, qp = (lane & 15) >> 2, p = lane & 3;
  const int bid = blockIdx.x;
  const int xcd = bid & 7, idx = bid >> 3;
  const int blk = xcd >> 2;
  const int hg = (xcd & 3) * 2 + (idx & 1);
  const int b = idx >> 1;
  const int h0 = hg * 4;
  const int u = wave >> 1, qh = wave & 1;

  const char* __restrict__ x = (const char*)(blk ? xB : xA) + (size_t)b * SQ * DM * 2;
  const char* __restrict__ kc = (const char*)(blk ? kB : kA) + ((size_t)b * HQ + h0) * (SQ * DH * 2);
  const char* __restrict__ vc = (const char*)(blk ? vB : vA) + ((size_t)b * HQ + h0) * (SQ * DH * 2);
  const char* __restrict__ wq = (const char*)(blk ? wqB : wqA) + h0 * DH * 2;
  const char* __restrict__ wo = (const char*)(blk ? woB : woA) + (size_t)h0 * DH * DM * 2;

  const unsigned lds0 = (unsigned)(uintptr_t)smem;
  STAMP(0);

  unsigned vw[2], vx[2], vk, vv;
#pragma unroll
  for (int e = 0; e < 2; ++e) {
    const int f = g | (e << 2);
    const int ch = (((c >> 1) ^ f) << 1) | (c & 1);
    vw[e] = (unsigned)((16 * qh + g) * (HD * 2) + u * (DH * 2) + ch * 16);
    const int r = 12 * wave + 6 * e + (lane >> 3);
    vx[e] = (unsigned)(r * (DM * 2) + (((lane & 7) ^ ((r >> 1) & 7)) << 4));
  }
  {
    const int row = 4 * wave + g;
    vk = (unsigned)(row * 256 + ((c ^ (row & 15)) << 4));
    vv = (unsigned)(row * 256 + (((((c >> 1) ^ (row & 7)) << 1) | (c & 1)) << 4));
  }

  auto issue_w = [&](int st) {
#pragma unroll
    for (int e = 0; e < 4; ++e)
      dma16(vw[e >> 1], wq + (size_t)(st * 32 + 4 * e) * (HD * 2), lds0 + (st % 3) * 32768 + (wave * 4 + e) * 1024);
  };
  auto issue_x = [&](int j) {
    if (lane < 48) {
#pragma unroll
      for (int e = 0; e < 2; ++e)
        dma16(vx[e], x + j * 128, lds0 + XBASE + (j & 1) * 12288 + (12 * wave + 6 * e) * 128);
    }
  };
  auto issue_piece = [&](int i) {
    const int kind = i / 12, uu = i & 3, pc = (i % 12) >> 2;
    dma16_nt(kind ? vv : vk, (kind ? vc : kc) + uu * (SQ * DH * 2) + pc * 8192, lds0 + piece_blk(i) * 8192 + wave * 1024);
  };

  const int fl = qp | ((g & 1) << 2);
  unsigned trw[8];
#pragma unroll
  for (int dt = 0; dt < 8; ++dt) trw[dt] = lds0 + u * 8192 + (8 * g + qp) * 256 + 8 * p + ((dt ^ fl) << 5);
  unsigned xrd[2];
#pragma unroll
  for (int hf = 0; hf < 2; ++hf)
    xrd[hf] = lds0 + XBASE + (qh * 48 + c) * 128 + (((4 * hf + g) ^ ((c >> 1) & 7)) << 4);

  f32x4 qacc[8][3];
  bf16x8 af[8], xf[3];
  auto read_a = [&](int st, int dt) {
    const unsigned a0 = trw[dt] + (st % 3) * 32768;
    return cat8(tr_read(a0), tr_read(a0 + 4 * 256));
  };
  auto read_x = [&](int st, int t) { return lds_frag(xrd[st & 1] + ((st >> 1) & 1) * 12288 + t * 16 * 128); };

  issue_w(0); issue_x(0); issue_w(1); issue_w(2); issue_x(1);
  wait_vm<10>();
  raw_barrier();
#pragma unroll
  for (int t = 0; t < 3; ++t) xf[t] = read_x(0, t);
#pragma unroll
  for (int dt = 0; dt < 8; ++dt) af[dt] = read_a(0, dt);

  auto step = [&](auto ic) {
    constexpr int s = decltype(ic)::value;
    if constexpr (s < 15) wait_vm<step_wait(s)>();
    raw_barrier();
    STAMP(1 + s);
    if constexpr (n_w(s) > 0) issue_w(s + 3);
    if constexpr (n_x(s) > 0) issue_x((s + 3) / 2);
#pragma unroll
    for (int i = first_piece(s); i < first_piece(s + 1); ++i) issue_piece(i);
    if constexpr (s == 0) {
      const int l = (wave - 3) * 64 + lane;
      const int xr = l < 288 ? l : 0;
      const unsigned o = wave < 3 ? (unsigned)((128 * (wave + 1) + 8 * b + (lane >> 3)) * (HD * 2) + (lane & 7) * 128)
                                  : (unsigned)(((idx & 1) * 48 + xr / 6) * (DM * 2) + (2 + xr % 6) * 128);
      touch(o, wave < 3 ? wq : x);
    }
    if constexpr (s == 8) touch((unsigned)(b * 32768 + wave * 4096 + (lane & 31) * 128), wo);
    bf16x8 xn[3], an[8];
    if constexpr (s < 15) {
#pragma unroll
      for (int t = 0; t < 3; ++t) xn[t] = read_x(s + 1, t);
      an[0] = read_a(s + 1, 0);
      an[1] = read_a(s + 1, 1);
    }
    __builtin_amdgcn_sched_barrier(0);
#pragma unroll
    for (int dt = 0; dt < 8; ++dt) {
#pragma unroll
      for (int t = 0; t < 3; ++t) {
        if constexpr (s == 0) mfma_init(qacc[dt][t], af[dt], xf[t]);
        else mfma_acc(qacc[dt][t], af[dt], xf[t]);
      }
      if constexpr (s < 15) {
        if (dt + 2 < 8) an[dt + 2] = read_a(s + 1, dt + 2);
      }
      __builtin_amdgcn_sched_barrier(0);
    }
    if constexpr (s < 15) {
#pragma unroll
      for (int t = 0; t < 3; ++t) xf[t] = xn[t];
#pragma unroll
      for (int dt = 0; dt < 8; ++dt) af[dt] = an[dt];
    }
  };
  step(IC<0>{}); step(IC<1>{}); step(IC<2>{}); step(IC<3>{}); step(IC<4>{}); step(IC<5>{}); step(IC<6>{}); step(IC<7>{});
  step(IC<8>{}); step(IC<9>{}); step(IC<10>{}); step(IC<11>{}); step(IC<12>{}); step(IC<13>{}); step(IC<14>{}); step(IC<15>{});

  asm volatile("s_nop 7\n\ts_nop 7" ::: "memory");
  __builtin_amdgcn_sched_barrier(0);
  STAMPW(17);
  bf16x8 qf[4][3];
#pragma unroll
  for (int s = 0; s < 4; ++s)
#pragma unroll
    for (int t = 0; t < 3; ++t) {
      u32x4 r;
#pragma unroll
      for (int e = 0; e < 2; ++e) {
        const unsigned a = pack2(qacc[2 * s][t][2 * e], qacc[2 * s][t][2 * e + 1]);
        const unsigned bq = pack2(qacc[2 * s + 1][t][2 * e], qacc[2 * s + 1][t][2 * e + 1]);
        auto r1 = __builtin_amdgcn_permlane32_swap(a, bq, false, false);
        auto r2 = __builtin_amdgcn_permlane16_swap(r1[0], r1[1], false, false);
        r[e] = r2[0];
        r[2 + e] = r2[1];
      }
      qf[s][t] = __builtin_bit_cast(bf16x8, r);
    }

  wait_vm<cnt(15) - pos_p(11)>();
  raw_barrier();
  STAMP(18);

  f32x4 sacc[6][3];
#pragma unroll
  for (int kt = 0; kt < 6; ++kt)
#pragma unroll
    for (int t = 0; t < 3; ++t) sacc[kt][t] = (f32x4){0.f, 0.f, 0.f, 0.f};
#pragma unroll
  for (int pc = 0; pc < 3; ++pc) {
    const unsigned kblk = (blk_tab(0, pc) >> (8 * u)) & 255u;
    const unsigned kb = lds0 + kblk * 8192 + c * 256;
#pragma unroll
    for (int k2 = 0; k2 < 2; ++k2) {
#pragma unroll
      for (int s = 0; s < 4; ++s) {
        const bf16x8 kf = lds_frag(kb + k2 * 4096 + (((4 * s + g) ^ c) << 4));
#pragma unroll
        for (int t = 0; t < 3; ++t) sacc[pc * 2 + k2][t] = MFMA16(kf, qf[s][t], sacc[pc * 2 + k2][t]);
      }
    }
  }
  STAMPW(19);
  raw_barrier();

  const unsigned ring = lds0 + (unsigned)((ring_tab() >> (8 * wave)) & 255u) * 8192;
  const int srow = lane >> 3, pos = lane & 7;
  const unsigned vo = (unsigned)(srow * (DM * 2) + (((((pos >> 1) ^ ((srow >> 1) & 3)) << 1) | (pos & 1)) << 4) + wave * 128);
  auto issue_wo = [&](int st, int e) {
    dma16(vo, wo + (size_t)(st * 32 + 8 * e) * (DM * 2), ring + (st & 1) * 4096 + e * 1024);
  };
#pragma unroll
  for (int i = 20; i < 24; ++i) issue_piece(i);
#pragma unroll
  for (int e = 0; e < 8; ++e) issue_wo(e >> 2, e & 3);

  const float cexp = 0.08838834764831845f * 1.4426950408889634f;
  bf16x8 pf[3][3];
  float rinv[3];
#pragma unroll
  for (int t = 0; t < 3; ++t) {
    float m = sacc[0][t][0];
#pragma unroll
    for (int kt = 0; kt < 6; ++kt)
#pragma unroll
      for (int r = 0; r < 4; ++r) m = fmaxf(m, sacc[kt][t][r]);
    m = xg_max(m);
    const float mc = m * cexp;
    float sum = 0.f;
#pragma unroll
    for (int kt = 0; kt < 6; ++kt)
#pragma unroll
      for (int r = 0; r < 4; ++r) {
        const float e = __builtin_amdgcn_exp2f(fmaf(sacc[kt][t][r], cexp, -mc));
        sacc[kt][t][r] = e;
        sum += e;
      }
    sum = xg_sum(sum);
    rinv[t] = 1.0f / sum;
#pragma unroll
    for (int s = 0; s < 3; ++s) {
      u32x4 r;
      r[0] = pack2(sacc[2 * s][t][0], sacc[2 * s][t][1]);
      r[1] = pack2(sacc[2 * s][t][2], sacc[2 * s][t][3]);
      r[2] = pack2(sacc[2 * s + 1][t][0], sacc[2 * s + 1][t][1]);
      r[3] = pack2(sacc[2 * s + 1][t][2], sacc[2 * s + 1][t][3]);
      pf[s][t] = __builtin_bit_cast(bf16x8, r);
    }
  }

  STAMPW(20);
  f32x4 oacc[8][3];
#pragma unroll
  for (int dt = 0; dt < 8; ++dt)
#pragma unroll
    for (int t = 0; t < 3; ++t) oacc[dt][t] = (f32x4){0.f, 0.f, 0.f, 0.f};
  const unsigned vlane = lds0 + (4 * g + qp) * 256 + 8 * p;
#pragma unroll
  for (int s = 0; s < 3; ++s) {
    if (s == 0) { wait_vm<12>(); raw_barrier(); }
    if (s == 2) { wait_vm<8>(); raw_barrier(); }
    const unsigned vblk = (blk_tab(1, s) >> (8 * u)) & 255u;
    const unsigned vb = vlane + vblk * 8192;
#pragma unroll
    for (int dt = 0; dt < 8; ++dt) {
      const unsigned a0 = vb + ((dt ^ fl) << 5);
      const bf16x8 vf = cat8(tr_read(a0), tr_read(a0 + 16 * 256));
#pragma unroll
      for (int t = 0; t < 3; ++t) oacc[dt][t] = MFMA16(vf, pf[s][t], oacc[dt][t]);
    }
  }
  STAMPW(21);
  raw_barrier();

#pragma unroll
  for (int s = 0; s < 4; ++s)
#pragma unroll
    for (int t = 0; t < 3; ++t) {
      const float ri = rinv[t];
      u32x4 r;
      r[0] = pack2(oacc[2 * s][t][0] * ri, oacc[2 * s][t][1] * ri);
      r[1] = pack2(oacc[2 * s][t][2] * ri, oacc[2 * s][t][3] * ri);
      r[2] = pack2(oacc[2 * s + 1][t][0] * ri, oacc[2 * s + 1][t][1] * ri);
      r[3] = pack2(oacc[2 * s + 1][t][2] * ri, oacc[2 * s + 1][t][3] * ri);
      const int f = s * 6 + qh * 3 + t;
      const unsigned tab = (f >> 3) == 0 ? blk_tab(1, 0) : (f >> 3) == 1 ? blk_tab(1, 1) : blk_tab(1, 2);
      const unsigned a = lds0 + ((tab >> (8 * u)) & 255u) * 8192 + (f & 7) * 1024 + lane * 16;
      *(lds_u32x4_ptr)(uintptr_t)a = r;
    }
  wait_vm<4>();
  raw_barrier();
  STAMP(22);

  const int fl2 = (2 * g + (qp >> 1)) & 3;
  unsigned wrd[4];
#pragma unroll
  for (int j = 0; j < 4; ++j) wrd[j] = ring + (4 * g + qp) * 128 + 8 * p + ((j ^ fl2) << 5);
  const unsigned olane = lds0 + lane * 16;
  auto read_wo = [&](int st, int j) {
    const unsigned a0 = wrd[j] + (st & 1) * 4096;
    return cat8(tr_read(a0), tr_read(a0 + 16 * 128));
  };
  auto read_o = [&](int st, int t) {
    const int f = (st & 3) * 6 + t;
    return lds_frag(olane + piece_blk(12 + (f >> 3) * 4 + (st >> 2)) * 8192 + (f & 7) * 1024);
  };
  f32x4 acc[4][6];
  bf16x8 wf[4], of[3];
#pragma unroll
  for (int j = 0; j < 4; ++j) wf[j] = read_wo(0, j);
  of[0] = read_o(0, 0);
  of[1] = read_o(0, 1);
  auto ostep = [&](auto ic) {
    constexpr int s = decltype(ic)::value;
    bf16x8 wn[4];
#pragma unroll
    for (int t = 0; t < 6; ++t) {
      if (s * 6 + t + 2 < 96) of[(t + 2) % 3] = read_o(s + (t + 2) / 6, (t + 2) % 6);
      if constexpr (s < 15) {
        if (t == 4) {
          if constexpr (s < 14) wait_vm<4>(); else wait_vm<0>();
#pragma unroll
          for (int j = 0; j < 4; ++j) wn[j] = read_wo(s + 1, j);
        }
      }
      __builtin_amdgcn_sched_barrier(0);
#pragma unroll
      for (int j = 0; j < 4; ++j) {
        if constexpr (s == 0) mfma_init(acc[j][t], wf[j], of[t % 3]);
        else mfma_acc(acc[j][t], wf[j], of[t % 3]);
      }
      if constexpr (s < 14) {
        if (t < 4) issue_wo(s + 2, t);
      }
      __builtin_amdgcn_sched_barrier(0);
    }
    if constexpr (s < 15) {
#pragma unroll
      for (int j = 0; j < 4; ++j) wf[j] = wn[j];
    }
  };
  ostep(IC<0>{}); ostep(IC<1>{}); ostep(IC<2>{}); ostep(IC<3>{}); ostep(IC<4>{}); ostep(IC<5>{}); ostep(IC<6>{}); ostep(IC<7>{});
  STAMPW(23);
  ostep(IC<8>{}); ostep(IC<9>{}); ostep(IC<10>{}); ostep(IC<11>{}); ostep(IC<12>{}); ostep(IC<13>{}); ostep(IC<14>{}); ostep(IC<15>{});
  asm volatile("s_nop 7\n\ts_nop 7" ::: "memory");
  __builtin_amdgcn_sched_barrier(0);
  STAMPW(24);

  u32x4* pw = p_ws + ((((size_t)(blk * NB + b) * 8 + hg) * 8 + wave) * 12) * 64 + lane;
#pragma unroll
  for (int t = 0; t < 6; ++t)
#pragma unroll
    for (int jp = 0; jp < 2; ++jp) {
      u32x4 r;
      r[0] = pack2(acc[2 * jp][t][0], acc[2 * jp][t][1]);
      r[1] = pack2(acc[2 * jp][t][2], acc[2 * jp][t][3]);
      r[2] = pack2(acc[2 * jp + 1][t][0], acc[2 * jp + 1][t][1]);
      r[3] = pack2(acc[2 * jp + 1][t][2], acc[2 * jp + 1][t][3]);
      pw[(t * 2 + jp) * 64] = r;
    }
  STAMPW(25);
  STAMPEND(26);
}

__global__ __launch_bounds__(768) void reduce_kernel(const u32x4* __restrict__ p_ws, unsigned short* __restrict__ out) {
  const int tid = threadIdx.x;
  const int lane = tid & 63, i = tid >> 6;
  const int bid = blockIdx.x;
  const int w = bid & 7, bb = bid >> 3;
  const u32x4* src = p_ws + (((size_t)bb * 8) * 8 + w) * 12 * 64 + tid;
  u32x4 v[8];
#pragma unroll
  for (int hg = 0; hg < 8; ++hg) v[hg] = src[(size_t)hg * 8 * 12 * 64];
  float s[8];
#pragma unroll
  for (int e = 0; e < 4; ++e) {
    float lo = 0.f, hi = 0.f;
#pragma unroll
    for (int hg = 0; hg < 8; ++hg) {
      lo += __uint_as_float(v[hg][e] << 16);
      hi += __uint_as_float(v[hg][e] & 0xffff0000u);
    }
    s[2 * e] = lo;
    s[2 * e + 1] = hi;
  }
  const int c = lane & 15, g = lane >> 4;
  const int q = (i >> 1) * 16 + c;
  const int n = w * 64 + (i & 1) * 32 + 4 * g;
  unsigned short* o = out + (size_t)((bb & 15) * SQ + q) * 1024 + (bb >> 4) * 512 + n;
  u32x2 r0, r1;
  r0[0] = pack2(s[0], s[1]); r0[1] = pack2(s[2], s[3]);
  r1[0] = pack2(s[4], s[5]); r1[1] = pack2(s[6], s[7]);
  *(u32x2*)o = r0;
  *(u32x2*)(o + 16) = r1;
}

}

extern "C" void kernel_launch(void* const* d_in, const int* in_sizes, int n_in, void* d_out, int out_size,
                              void* d_ws, size_t ws_size, hipStream_t stream) {
  const short* xA = (const short*)d_in[0];
  const short* kA = (const short*)d_in[1];
  const short* vA = (const short*)d_in[2];
  const short* wqA = (const short*)d_in[3];
  const short* woA = (const short*)d_in[4];
  const short* xB = (const short*)d_in[5];
  const short* kB = (const short*)d_in[6];
  const short* vB = (const short*)d_in[7];
  const short* wqB = (const short*)d_in[8];
  const short* woB = (const short*)d_in[9];
  u32x4* p_ws = (u32x4*)d_ws;
  (void)hipFuncSetAttribute((const void*)fused_kernel, hipFuncAttributeMaxDynamicSharedMemorySize, K1_LDS);
  hipLaunchKernelGGL(fused_kernel, dim3(256), dim3(512), K1_LDS, stream, xA, kA, vA, wqA, woA, xB, kB, vB, wqB,
                     woB, p_ws);
  hipLaunchKernelGGL(reduce_kernel, dim3(256), dim3(768), 0, stream, (const u32x4*)p_ws, (unsigned short*)d_out);
}
```

```cpp
#include <hip/hip_runtime.h>
#include <hip/hip_bf16.h>
#include <stdint.h>

typedef __attribute__((ext_vector_type(8))) short bf16x8;
typedef __attribute__((ext_vector_type(4))) short s16x4;
typedef __attribute__((ext_vector_type(4))) float f32x4;
typedef __attribute__((ext_vector_type(2))) float f32x2;
typedef __attribute__((ext_vector_type(4))) unsigned u32x4;
typedef __attribute__((ext_vector_type(2))) unsigned u32x2;
typedef __attribute__((ext_vector_type(2))) __bf16 bf16x2_t;
typedef __attribute__((address_space(3))) s16x4* lds_s16x4_ptr;

#define DI __device__ __forceinline__
#define MFMA16(a, b, c) __builtin_amdgcn_mfma_f32_16x16x32_bf16((a), (b), (c), 0, 0, 0)

#define STAMP(i)
#define STAMPW(i)
#define STAMPEND(i)
namespace {

constexpr int NB = 16, SQ = 96, DM = 512, HQ = 32, DH = 128, HD = HQ * DH;

DI unsigned pack2(float a, float b) {
  f32x2 v = {a, b};
  return __builtin_bit_cast(unsigned, __builtin_convertvector(v, bf16x2_t));
}
DI s16x4 tr_read(unsigned lds_addr) {
  return __builtin_amdgcn_ds_read_tr16_b64_v4i16((lds_s16x4_ptr)(uintptr_t)lds_addr);
}
DI bf16x8 cat8(s16x4 lo, s16x4 hi) { return __builtin_shufflevector(lo, hi, 0, 1, 2, 3, 4, 5, 6, 7); }
DI float xg_max(float x) {
  auto s = __builtin_amdgcn_permlane16_swap(__float_as_uint(x), __float_as_uint(x), false, false);
  x = fmaxf(__uint_as_float(s[0]), __uint_as_float(s[1]));
  auto t = __builtin_amdgcn_permlane32_swap(__float_as_uint(x), __float_as_uint(x), false, false);
  return fmaxf(__uint_as_float(t[0]), __uint_as_float(t[1]));
}
DI float xg_sum(float x) {
  auto s = __builtin_amdgcn_permlane16_swap(__float_as_uint(x), __float_as_uint(x), false, false);
  x = __uint_as_float(s[0]) + __uint_as_float(s[1]);
  auto t = __builtin_amdgcn_permlane32_swap(__float_as_uint(x), __float_as_uint(x), false, false);
  return __uint_as_float(t[0]) + __uint_as_float(t[1]);
}

constexpr int K1_LDS = 160 * 1024;
constexpr int XBASE = 96 * 1024;

constexpr int piece_blk(int i) {
  constexpr int tb[24] = {15, 16, 17, 18, 19, 4, 5, 6, 7, 12, 8, 9,
                          15, 16, 17, 18, 10, 11, 2, 3, 0, 1, 13, 14};
  return tb[i];
}
constexpr unsigned blk_tab(int kind, int pc) {
  unsigned r = 0;
  for (int u = 0; u < 4; ++u) r |= (unsigned)piece_blk(kind * 12 + pc * 4 + u) << (8 * u);
  return r;
}
constexpr int ring_blk(int w) {
  return piece_blk(4 + ((w >> 1) & 1) * 4 + (w >> 2) * 2 + (w & 1));
}
constexpr unsigned long long ring_tab() {
  unsigned long long r = 0;
  for (int w = 0; w < 8; ++w) r |= (unsigned long long)ring_blk(w) << (8 * w);
  return r;
}

constexpr int n_w(int s) { return (s + 3 <= 15) ? 8 : 0; }
constexpr int n_x(int s) { return ((s & 1) && ((s + 3) / 2 <= 7)) ? 3 : 0; }
constexpr int n0(int s) { return n_w(s) + (s >= 13 ? 4 : 0); }
constexpr int cnt0(int s) {
  int n = 11;
  for (int j = 0; j <= s; ++j) n += n0(j);
  return n;
}
constexpr int pos_w(int st) { return st <= 2 ? 11 : cnt0(st - 4) + 8; }
constexpr int cmax(int a, int b) { return a > b ? a : b; }
constexpr int wait0(int s) { return cnt0(s - 1) - pos_w(s + 1); }
constexpr int n1(int s) {
  return n_x(s) + (s >= 1 && s <= 12 ? 2 : 0) + (s <= 9 ? 1 : 0) + (s == 13 ? 4 : s == 15 ? 8 : 0);
}
constexpr int cnt1(int s) {
  int n = 21;
  for (int j = 0; j <= s; ++j) n += n1(j);
  return n;
}
constexpr int pos_k(int i) { return cnt1(i) + n_x(i + 1) + 2; }
constexpr int wait1(int s) {
  int need = ((s & 1) && s >= 3 && s < 15) ? cnt1(s - 3) + 3 : s == 1 ? 20 : 0;
  if (s == 13) need = cmax(need, pos_k(9));
  if (s == 14) need = cmax(need, pos_k(11));
  return need ? cnt1(s - 1) - need : -1;
}

template <int N> DI void wait_vm() { asm volatile("s_waitcnt vmcnt(%0)" ::"n"(N) : "memory"); }
DI void raw_barrier() {
  asm volatile("s_waitcnt lgkmcnt(0)" ::: "memory");
  __builtin_amdgcn_s_barrier();
  asm volatile("" ::: "memory");
}
DI void dma16(unsigned voff, const void* sbase, unsigned lds_dst) {
  unsigned keep;
  asm volatile("s_mov_b32 %0, m0\n\ts_mov_b32 m0, %3\n\ts_nop 0\n\tglobal_load_lds_dwordx4 %1, %2\n\ts_mov_b32 m0, %0"
               : "=&s"(keep) : "v"(voff), "s"(sbase), "s"(lds_dst) : "memory");
}
DI void dma16_nt(unsigned voff, const void* sbase, unsigned lds_dst) {
  unsigned keep;
  asm volatile("s_mov_b32 %0, m0\n\ts_mov_b32 m0, %3\n\ts_nop 0\n\tglobal_load_lds_dwordx4 %1, %2 nt\n\ts_mov_b32 m0, %0"
               : "=&s"(keep) : "v"(voff), "s"(sbase), "s"(lds_dst) : "memory");
}
template <int LO, int HI> DI void touch(const char* a) {
  asm volatile("s_mov_b32 exec_lo, %1\n\ts_mov_b32 exec_hi, %2\n\tglobal_load_dword v255, %0, off\n\ts_mov_b64 exec, -1" ::"v"(a), "n"(LO), "n"(HI) : "v255", "memory");
}
DI void dma16_48(unsigned voff, const void* sbase, unsigned lds_dst) {
  unsigned keep;
  asm volatile("s_mov_b32 %0, m0\n\ts_mov_b32 m0, %3\n\ts_mov_b32 exec_hi, 0xffff\n\tglobal_load_lds_dwordx4 %1, %2\n\ts_mov_b32 exec_hi, -1\n\ts_mov_b32 m0, %0"
               : "=&s"(keep) : "v"(voff), "s"(sbase), "s"(lds_dst) : "memory");
}
DI void gload_nt(u32x4& d, unsigned voff, const void* sbase) {
  asm volatile("global_load_dwordx4 %0, %1, %2 nt" : "=v"(d) : "v"(voff), "s"(sbase) : "memory");
}
DI void mfma_acc(f32x4& c, bf16x8 a, bf16x8 b) {
  asm volatile("v_mfma_f32_16x16x32_bf16 %0, %1, %2, %0" : "+v"(c) : "v"(a), "v"(b));
}
DI void mfma_init(f32x4& c, bf16x8 a, bf16x8 b) {
  asm volatile("v_mfma_f32_16x16x32_bf16 %0, %1, %2, 0" : "=&v"(c) : "v"(a), "v"(b));
}
template <int V> struct IC { static constexpr int value = V; };
typedef const __attribute__((address_space(3))) bf16x8* lds_frag_ptr;
typedef __attribute__((address_space(3))) u32x4* lds_u32x4_ptr;
DI bf16x8 lds_frag(unsigned a) { return *(lds_frag_ptr)(uintptr_t)a; }

__global__ __launch_bounds__(512, 2) void fused_kernel(
    const short* __restrict__ xA, const short* __restrict__ kA, const short* __restrict__ vA,
    const short* __restrict__ wqA, const short* __restrict__ woA, const short* __restrict__ xB,
    const short* __restrict__ kB, const short* __restrict__ vB, const short* __restrict__ wqB,
    const short* __restrict__ woB, u32x4* __restrict__ p_ws) {
  extern __shared__ __attribute__((aligned(16))) char smem[];
  const int tid = threadIdx.x;
  const int lane = tid & 63;
  const int wave = __builtin_amdgcn_readfirstlane(tid >> 6);
  const int c = lane & 15, g = lane >> 4, qp = (lane & 15) >> 2, p = lane & 3;
  const int bid = blockIdx.x;
  const int xcd = bid & 7, idx = bid >> 3;
  const int blk = xcd >> 2;
  const int hg = (xcd & 3) * 2 + (idx & 1);
  const int b = idx >> 1;
  const int h0 = hg * 4;
  const int u = wave >> 1, qh = wave & 1;

  const char* __restrict__ x = (const char*)(blk ? xB : xA) + (size_t)b * SQ * DM * 2;
  const char* __restrict__ kc = (const char*)(blk ? kB : kA) + ((size_t)b * HQ + h0) * (SQ * DH * 2);
  const char* __restrict__ vc = (const char*)(blk ? vB : vA) + ((size_t)b * HQ + h0) * (SQ * DH * 2);
  const char* __restrict__ wq = (const char*)(blk ? wqB : wqA) + h0 * DH * 2;
  const char* __restrict__ wo = (const char*)(blk ? woB : woA) + (size_t)h0 * DH * DM * 2;

  const unsigned lds0 = (unsigned)(uintptr_t)smem;
  STAMP(0);

  const int w4 = wave & 3;
  unsigned vw[2], vx[2], vk[2], vv[2];
#pragma unroll
  for (int e = 0; e < 2; ++e) {
    const int f = g | (e << 2);
    const int ch = (((c >> 1) ^ f) << 1) | (c & 1);
    vw[e] = (unsigned)(g * (HD * 2) + w4 * (DH * 2) + ch * 16);
    const int r = 24 * w4 + 8 * e + (lane >> 3);
    vx[e] = (unsigned)((24 * w4 + (lane >> 3)) * (DM * 2) + (((lane & 7) ^ ((r >> 1) & 7)) << 4));
    const int row = 8 * w4 + 4 * e + g;
    vk[e] = (unsigned)(row * 256 + ((c ^ (row & 15)) << 4));
    vv[e] = (unsigned)(row * 256 + (((((c >> 1) ^ (row & 7)) << 1) | (c & 1)) << 4));
  }

  auto issue_w = [&](int st, int e) {
    dma16(vw[(e >> 1) & 1], wq + (size_t)(st * 32 + 4 * e) * (HD * 2), lds0 + (st % 3) * 32768 + (w4 * 8 + e) * 1024);
  };
  auto issue_x = [&](int j, int e) {
    dma16(vx[e & 1], x + j * 128 + e * 8 * (DM * 2), lds0 + XBASE + (j & 1) * 12288 + (24 * w4 + 8 * e) * 128);
  };
  auto piece_src = [&](int i) { return (i >= 12 ? vc : kc) + (i & 3) * (SQ * DH * 2) + ((i % 12) >> 2) * 8192; };
  auto piece_dst = [&](int i, int e) { return lds0 + piece_blk(i) * 8192 + (w4 * 2 + e) * 1024; };
  auto dma_piece = [&](int i, int e) { dma16_nt(i >= 12 ? vv[e] : vk[e], piece_src(i), piece_dst(i, e)); };

  const int fl = qp | ((g & 1) << 2);
  unsigned trw[8];
#pragma unroll
  for (int dt = 0; dt < 8; ++dt) trw[dt] = lds0 + u * 8192 + (8 * g + qp) * 256 + 8 * p + ((dt ^ fl) << 5);
  unsigned xrd[2];
#pragma unroll
  for (int hf = 0; hf < 2; ++hf)
    xrd[hf] = lds0 + XBASE + (qh * 48 + c) * 128 + (((4 * hf + g) ^ ((c >> 1) & 7)) << 4);
  auto read_a = [&](int st, int dt) {
    const unsigned a0 = trw[dt] + (st % 3) * 32768;
    return cat8(tr_read(a0), tr_read(a0 + 4 * 256));
  };
  auto read_x = [&](int st, int t) { return lds_frag(xrd[st & 1] + ((st >> 1) & 1) * 12288 + t * 16 * 128); };

  const unsigned ring = lds0 + (unsigned)((ring_tab() >> (8 * wave)) & 255u) * 8192;
  unsigned vo;
  auto issue_wo = [&](int st, int e) {
    dma16(vo, wo + (size_t)(st * 32 + 8 * e) * (DM * 2), ring + (st & 1) * 4096 + e * 1024);
  };
  auto front = [&](auto rc) {
    constexpr int ROLE = decltype(rc)::value;
    f32x4 qacc[8][3];
    bf16x8 af[8], xf[3];
    u32x4 kst[14], vst[4];

    const int tl = lane * 4 + w4;
    const char* ta = wq + (size_t)((tl >> 4) * 32 + 2 * b + ((tl >> 3) & 1)) * (HD * 2) + (tl & 7) * 128;
    if constexpr (ROLE == 0) {
#pragma unroll
      for (int e = 0; e < 3; ++e) issue_x(0, e);
#pragma unroll
      for (int e = 0; e < 8; ++e) issue_w(0, e);
      wait_vm<0>();
    } else {
      touch<0xffff0, 0>(ta);
      wait_vm<0>();
#pragma unroll
      for (int e = 8; e < 24; ++e) issue_w(e >> 3, e & 7);
#pragma unroll
      for (int e = 0; e < 3; ++e) issue_x(1, e);
      touch<0xf00000, 0>(ta);
    }
    raw_barrier();
    STAMP(1);
    STAMPW(2);
#pragma unroll
    for (int t = 0; t < 3; ++t) xf[t] = read_x(0, t);
#pragma unroll
    for (int dt = 0; dt < 8; ++dt) af[dt] = read_a(0, dt);

    auto mem_op = [&](auto sc, auto kc_) {
      constexpr int s = decltype(sc)::value, k = decltype(kc_)::value;
      if constexpr (ROLE == 0) {
        if constexpr (k < n_w(s)) issue_w(s + 3, k);
        else if constexpr (s == 13 && k < 4) gload_nt(vst[k], vv[k & 1], piece_src(12 + (k >> 1)));
        else if constexpr (s == 14 && k < 4) dma_piece(16 + (k >> 1), k & 1);
        else if constexpr (s == 15 && k < 4) dma_piece(20 + (k >> 1), k & 1);
      } else {
        constexpr int k2 = k - n_x(s);
        if constexpr (k2 < 0) {
          issue_x((s + 3) / 2, k);
        } else if constexpr (s >= 1 && s <= 12 && k2 < 2) {
          if constexpr (s - 1 < 5) dma_piece(s - 1, k2);
          else gload_nt(kst[(s - 6) * 2 + k2], vk[k2], piece_src(s - 1));
        } else if constexpr (s <= 9 && k2 == (s == 0 ? 0 : 2)) {
          constexpr unsigned long long m = 15ull << (4 * (s + 6));
          touch<(int)(unsigned)m, (int)(unsigned)(m >> 32)>(ta);
        } else if constexpr (s == 13 && k < 10) {
          *(lds_u32x4_ptr)(uintptr_t)(piece_dst(5 + (k >> 1), k & 1) + lane * 16) = kst[k];
        } else if constexpr (s == 13 && k < 14) {
          gload_nt(vst[k - 10], vv[k & 1], piece_src(14 + ((k - 10) >> 1)));
        } else if constexpr (s == 14 && k < 4) {
          *(lds_u32x4_ptr)(uintptr_t)(piece_dst(10 + (k >> 1), k & 1) + lane * 16) = kst[10 + k];
        } else if constexpr (s == 15 && k < 8) {
          dma_piece(18 + (k >> 1) + (k >= 4 ? 2 : 0), k & 1);
        }
      }
    };
    auto group = [&](auto sc, auto dc, bf16x8* an, bf16x8* xn) {
      constexpr int s = decltype(sc)::value, dt = decltype(dc)::value;
      constexpr int n = ROLE == 0 ? n0(s) : (s == 13 ? 14 : s == 14 ? 4 : n1(s));
      constexpr int per = n <= 8 ? 1 : n <= 16 ? 2 : 3;
#pragma unroll
      for (int t = 0; t < 3; ++t) {
        if constexpr (s == 0) mfma_init(qacc[dt][t], af[dt], xf[t]);
        else mfma_acc(qacc[dt][t], af[dt], xf[t]);
      }
      if constexpr (s > 0 && s < 15) {
        if constexpr (dt == 0) { an[0] = read_a(s + 1, 0); an[1] = read_a(s + 1, 1); }
        if constexpr (dt >= 1 && dt <= 3) xn[dt - 1] = read_x(s + 1, dt - 1);
        if constexpr (dt >= 1 && dt < 7) an[dt + 1] = read_a(s + 1, dt + 1);
      }
      mem_op(sc, IC<dt * per>{});
      if constexpr (per > 1) mem_op(sc, IC<dt * per + 1>{});
      if constexpr (per > 2) mem_op(sc, IC<dt * per + 2>{});
      __builtin_amdgcn_sched_barrier(0);
    };
    auto step = [&](auto ic) {
      constexpr int s = decltype(ic)::value;
      if constexpr (ROLE == 0 && s > 1 && s < 15) wait_vm<wait0(s)>();
      if constexpr (ROLE == 1 && wait1(s) >= 0) wait_vm<wait1(s)>();
      raw_barrier();
      STAMP(3 + s);
      bf16x8 xn[3], an[8];
      group(ic, IC<0>{}, an, xn); group(ic, IC<1>{}, an, xn); group(ic, IC<2>{}, an, xn); group(ic, IC<3>{}, an, xn);
      group(ic, IC<4>{}, an, xn); group(ic, IC<5>{}, an, xn); group(ic, IC<6>{}, an, xn); group(ic, IC<7>{}, an, xn);
      if constexpr (s == 0) {
        if constexpr (ROLE == 1) wait_vm<cnt1(0) - 9>();
        raw_barrier();
#pragma unroll
        for (int t = 0; t < 3; ++t) xn[t] = read_x(1, t);
#pragma unroll
        for (int dt = 0; dt < 8; ++dt) an[dt] = read_a(1, dt);
      }
      if constexpr (s < 15) {
#pragma unroll
        for (int t = 0; t < 3; ++t) xf[t] = xn[t];
#pragma unroll
        for (int dt = 0; dt < 8; ++dt) af[dt] = an[dt];
      }
    };
    step(IC<0>{}); step(IC<1>{}); step(IC<2>{}); step(IC<3>{}); step(IC<4>{}); step(IC<5>{}); step(IC<6>{}); step(IC<7>{});
    step(IC<8>{}); step(IC<9>{}); step(IC<10>{}); step(IC<11>{}); step(IC<12>{}); step(IC<13>{}); step(IC<14>{}); step(IC<15>{});

    asm volatile("s_nop 7\n\ts_nop 7" ::: "memory");
    __builtin_amdgcn_sched_barrier(0);
    STAMPW(19);
    bf16x8 qf[4][3];
#pragma unroll
    for (int s = 0; s < 4; ++s)
#pragma unroll
      for (int t = 0; t < 3; ++t) {
        u32x4 r;
#pragma unroll
        for (int e = 0; e < 2; ++e) {
          const unsigned a = pack2(qacc[2 * s][t][2 * e], qacc[2 * s][t][2 * e + 1]);
          const unsigned bq = pack2(qacc[2 * s + 1][t][2 * e], qacc[2 * s + 1][t][2 * e + 1]);
          auto r1 = __builtin_amdgcn_permlane32_swap(a, bq, false, false);
          auto r2 = __builtin_amdgcn_permlane16_swap(r1[0], r1[1], false, false);
          r[e] = r2[0];
          r[2 + e] = r2[1];
        }
        qf[s][t] = __builtin_bit_cast(bf16x8, r);
      }
    STAMPW(20);
    int lane1 = threadIdx.x & 63;
    asm volatile("" : "+v"(lane1));
    const int c1 = lane1 & 15, g1 = lane1 >> 4, qp1 = (lane1 & 15) >> 2, p1 = lane1 & 3;
    const int fl1 = qp1 | ((g1 & 1) << 2);
    if constexpr (ROLE == 1) raw_barrier();

    f32x4 sacc[6][3];
#pragma unroll
    for (int kt = 0; kt < 6; ++kt)
#pragma unroll
      for (int t = 0; t < 3; ++t) sacc[kt][t] = (f32x4){0.f, 0.f, 0.f, 0.f};
#pragma unroll
    for (int pc = 0; pc < 3; ++pc) {
      const unsigned kblk = (blk_tab(0, pc) >> (8 * u)) & 255u;
      const unsigned kb = lds0 + kblk * 8192 + c1 * 256;
#pragma unroll
      for (int k2 = 0; k2 < 2; ++k2) {
#pragma unroll
        for (int s = 0; s < 4; ++s) {
          const bf16x8 kf = lds_frag(kb + k2 * 4096 + (((4 * s + g1) ^ c1) << 4));
#pragma unroll
          for (int t = 0; t < 3; ++t) sacc[pc * 2 + k2][t] = MFMA16(kf, qf[s][t], sacc[pc * 2 + k2][t]);
        }
      }
    }
    STAMPW(21);
    wait_vm<8>();
    raw_barrier();
#pragma unroll
    for (int k = 0; k < 4; ++k)
      *(lds_u32x4_ptr)(uintptr_t)(piece_dst(12 + 2 * ROLE + (k >> 1), k & 1) + lane1 * 16) = vst[k];
    __builtin_amdgcn_sched_barrier(0);

    {
      const int srow = lane1 >> 3, pos = lane1 & 7;
      vo = (unsigned)(srow * (DM * 2) + (((((pos >> 1) ^ ((srow >> 1) & 3)) << 1) | (pos & 1)) << 4) + wave * 128);
    }
#pragma unroll
    for (int e = 0; e < 8; ++e) issue_wo(e >> 2, e & 3);
    {
      const int tw = 32 + (lane1 & 31) * 8 + wave;
      touch<0xfffffff, 0>(wo + ((tw >> 4) * 32 + 2 * b + ((tw >> 3) & 1)) * (DM * 2) + (tw & 7) * 128);
    }

    const float cexp = 0.08838834764831845f * 1.4426950408889634f;
    bf16x8 pf[3][3];
    float rinv[3];
#pragma unroll
    for (int t = 0; t < 3; ++t) {
      float m = sacc[0][t][0];
#pragma unroll
      for (int kt = 0; kt < 6; ++kt)
#pragma unroll
        for (int r = 0; r < 4; ++r) m = fmaxf(m, sacc[kt][t][r]);
      m = xg_max(m);
      const float mc = m * cexp;
      float sum = 0.f;
#pragma unroll
      for (int kt = 0; kt < 6; ++kt)
#pragma unroll
        for (int r = 0; r < 4; ++r) {
          const float e = __builtin_amdgcn_exp2f(fmaf(sacc[kt][t][r], cexp, -mc));
          sacc[kt][t][r] = e;
          sum += e;
        }
      sum = xg_sum(sum);
      rinv[t] = 1.0f / sum;
#pragma unroll
      for (int s = 0; s < 3; ++s) {
        u32x4 r;
        r[0] = pack2(sacc[2 * s][t][0], sacc[2 * s][t][1]);
        r[1] = pack2(sacc[2 * s][t][2], sacc[2 * s][t][3]);
        r[2] = pack2(sacc[2 * s + 1][t][0], sacc[2 * s + 1][t][1]);
        r[3] = pack2(sacc[2 * s + 1][t][2], sacc[2 * s + 1][t][3]);
        pf[s][t] = __builtin_bit_cast(bf16x8, r);
      }
    }
    STAMPW(22);
    wait_vm<9>();
    raw_barrier();

    f32x4 oacc[8][3];
#pragma unroll
    for (int dt = 0; dt < 8; ++dt)
#pragma unroll
      for (int t = 0; t < 3; ++t) oacc[dt][t] = (f32x4){0.f, 0.f, 0.f, 0.f};
    const unsigned vlane = lds0 + (4 * g1 + qp1) * 256 + 8 * p1;
#pragma unroll
    for (int s = 0; s < 3; ++s) {
      const unsigned vblk = (blk_tab(1, s) >> (8 * u)) & 255u;
      const unsigned vb = vlane + vblk * 8192;
#pragma unroll
      for (int dt = 0; dt < 8; ++dt) {
        const unsigned a0 = vb + ((dt ^ fl1) << 5);
        const bf16x8 vf = cat8(tr_read(a0), tr_read(a0 + 16 * 256));
#pragma unroll
        for (int t = 0; t < 3; ++t) oacc[dt][t] = MFMA16(vf, pf[s][t], oacc[dt][t]);
      }
    }
    STAMPW(23);
    raw_barrier();

#pragma unroll
    for (int s = 0; s < 4; ++s)
#pragma unroll
      for (int t = 0; t < 3; ++t) {
        const float ri = rinv[t];
        u32x4 r;
        r[0] = pack2(oacc[2 * s][t][0] * ri, oacc[2 * s][t][1] * ri);
        r[1] = pack2(oacc[2 * s][t][2] * ri, oacc[2 * s][t][3] * ri);
        r[2] = pack2(oacc[2 * s + 1][t][0] * ri, oacc[2 * s + 1][t][1] * ri);
        r[3] = pack2(oacc[2 * s + 1][t][2] * ri, oacc[2 * s + 1][t][3] * ri);
        const int f = s * 6 + qh * 3 + t;
        const unsigned tab = (f >> 3) == 0 ? blk_tab(1, 0) : (f >> 3) == 1 ? blk_tab(1, 1) : blk_tab(1, 2);
        const unsigned a = lds0 + ((tab >> (8 * u)) & 255u) * 8192 + (f & 7) * 1024 + lane1 * 16;
        *(lds_u32x4_ptr)(uintptr_t)a = r;
      }
    wait_vm<5>();
    raw_barrier();
    STAMP(24);
  };
  if (wave < 4) front(IC<0>{}); else front(IC<1>{});
  {
  int lane = threadIdx.x & 63;
  asm volatile("" : "+v"(lane));
  const int g = lane >> 4, qp = (lane & 15) >> 2, p = lane & 3;
  unsigned ring_o = ring;
  asm volatile("" : "+s"(ring_o));
  auto issue_wo = [&](int st, int e) {
    dma16(vo, wo + (size_t)(st * 32 + 8 * e) * (DM * 2), ring_o + (st & 1) * 4096 + e * 1024);
  };

  const int fl2 = (2 * g + (qp >> 1)) & 3;
  unsigned wrd[4];
#pragma unroll
  for (int j = 0; j < 4; ++j) wrd[j] = ring_o + (4 * g + qp) * 128 + 8 * p + ((j ^ fl2) << 5);
  const unsigned olane = lds0 + lane * 16;
  auto read_wo = [&](int st, int j) {
    const unsigned a0 = wrd[j] + (st & 1) * 4096;
    return cat8(tr_read(a0), tr_read(a0 + 16 * 128));
  };
  auto read_o = [&](int st, int t) {
    const int f = (st & 3) * 6 + t;
    return lds_frag(olane + piece_blk(12 + (f >> 3) * 4 + (st >> 2)) * 8192 + (f & 7) * 1024);
  };
  f32x4 acc[4][6];
  bf16x8 wf[4], of[3];
#pragma unroll
  for (int j = 0; j < 4; ++j) wf[j] = read_wo(0, j);
  of[0] = read_o(0, 0);
  of[1] = read_o(0, 1);
  auto ostep = [&](auto ic) {
    constexpr int s = decltype(ic)::value;
    if constexpr (s == 1) { if (wave < 4) __builtin_amdgcn_s_barrier(); }
    bf16x8 wn[4];
#pragma unroll
    for (int t = 0; t < 6; ++t) {
      if (s * 6 + t + 2 < 96) of[(t + 2) % 3] = read_o(s + (t + 2) / 6, (t + 2) % 6);
      if constexpr (s < 15) {
        if (t == 4) {
          if constexpr (s < 14) wait_vm<4>(); else wait_vm<0>();
#pragma unroll
          for (int j = 0; j < 4; ++j) wn[j] = read_wo(s + 1, j);
        }
      }
      __builtin_amdgcn_sched_barrier(0);
#pragma unroll
      for (int j = 0; j < 4; ++j) {
        if constexpr (s == 0) mfma_init(acc[j][t], wf[j], of[t % 3]);
        else mfma_acc(acc[j][t], wf[j], of[t % 3]);
      }
      if constexpr (s < 14) {
        if (t < 4) issue_wo(s + 2, t);
      }
      __builtin_amdgcn_sched_barrier(0);
    }
    if constexpr (s < 15) {
#pragma unroll
      for (int j = 0; j < 4; ++j) wf[j] = wn[j];
    }
  };
  ostep(IC<0>{}); ostep(IC<1>{}); ostep(IC<2>{}); ostep(IC<3>{}); ostep(IC<4>{}); ostep(IC<5>{}); ostep(IC<6>{}); ostep(IC<7>{});
  STAMPW(25);
  ostep(IC<8>{}); ostep(IC<9>{}); ostep(IC<10>{}); ostep(IC<11>{}); ostep(IC<12>{}); ostep(IC<13>{}); ostep(IC<14>{}); ostep(IC<15>{});
  asm volatile("s_nop 7\n\ts_nop 7" ::: "memory");
  __builtin_amdgcn_sched_barrier(0);
  STAMPW(26);

  u32x4* pw = p_ws + ((((size_t)(blk * NB + b) * 8 + hg) * 8 + wave) * 12) * 64 + lane;
#pragma unroll
  for (int t = 0; t < 6; ++t)
#pragma unroll
    for (int jp = 0; jp < 2; ++jp) {
      u32x4 r;
      r[0] = pack2(acc[2 * jp][t][0], acc[2 * jp][t][1]);
      r[1] = pack2(acc[2 * jp][t][2], acc[2 * jp][t][3]);
      r[2] = pack2(acc[2 * jp + 1][t][0], acc[2 * jp + 1][t][1]);
      r[3] = pack2(acc[2 * jp + 1][t][2], acc[2 * jp + 1][t][3]);
      asm volatile("global_store_dwordx4 %0, %1, off sc1\n\ts_nop 1" ::"v"(pw + (t * 2 + jp) * 64), "v"(r) : "memory");
    }
  STAMPW(27);
  STAMPEND(28);
  }
}

__global__ __launch_bounds__(768) void reduce_kernel(const u32x4* __restrict__ p_ws, unsigned short* __restrict__ out) {
  const int tid = threadIdx.x;
  const int lane = tid & 63, i = tid >> 6;
  const int bid = blockIdx.x;
  const int w = bid & 7, bb = bid >> 3;
  const u32x4* src = p_ws + (((size_t)bb * 8) * 8 + w) * 12 * 64 + tid;
  u32x4 v[8];
#pragma unroll
  for (int hg = 0; hg < 8; ++hg) v[hg] = src[(size_t)hg * 8 * 12 * 64];
  float s[8];
#pragma unroll
  for (int e = 0; e < 4; ++e) {
    float lo = 0.f, hi = 0.f;
#pragma unroll
    for (int hg = 0; hg < 8; ++hg) {
      lo += __uint_as_float(v[hg][e] << 16);
      hi += __uint_as_float(v[hg][e] & 0xffff0000u);
    }
    s[2 * e] = lo;
    s[2 * e + 1] = hi;
  }
  const int c = lane & 15, g = lane >> 4;
  const int q = (i >> 1) * 16 + c;
  const int n = w * 64 + (i & 1) * 32 + 4 * g;
  unsigned short* o = out + (size_t)((bb & 15) * SQ + q) * 1024 + (bb >> 4) * 512 + n;
  u32x2 r0, r1;
  r0[0] = pack2(s[0], s[1]); r0[1] = pack2(s[2], s[3]);
  r1[0] = pack2(s[4], s[5]); r1[1] = pack2(s[6], s[7]);
  *(u32x2*)o = r0;
  *(u32x2*)(o + 16) = r1;
}

}

extern "C" void kernel_launch(void* const* d_in, const int* in_sizes, int n_in, void* d_out, int out_size,
                              void* d_ws, size_t ws_size, hipStream_t stream) {
  const short* xA = (const short*)d_in[0];
  const short* kA = (const short*)d_in[1];
  const short* vA = (const short*)d_in[2];
  const short* wqA = (const short*)d_in[3];
  const short* woA = (const short*)d_in[4];
  const short* xB = (const short*)d_in[5];
  const short* kB = (const short*)d_in[6];
  const short* vB = (const short*)d_in[7];
  const short* wqB = (const short*)d_in[8];
  const short* woB = (const short*)d_in[9];
  u32x4* p_ws = (u32x4*)d_ws;
  (void)hipFuncSetAttribute((const void*)fused_kernel, hipFuncAttributeMaxDynamicSharedMemorySize, K1_LDS);
  hipLaunchKernelGGL(fused_kernel, dim3(256), dim3(512), K1_LDS, stream, xA, kA, vA, wqA, woA, xB, kB, vB, wqB,
                     woB, p_ws);
  hipLaunchKernelGGL(reduce_kernel, dim3(256), dim3(768), 0, stream, (const u32x4*)p_ws, (unsigned short*)d_out);
}
```

```cpp
#include <hip/hip_runtime.h>
#include <hip/hip_bf16.h>
#include <stdint.h>

typedef __attribute__((ext_vector_type(8))) short bf16x8;
typedef __attribute__((ext_vector_type(4))) short s16x4;
typedef __attribute__((ext_vector_type(4))) float f32x4;
typedef __attribute__((ext_vector_type(2))) float f32x2;
typedef __attribute__((ext_vector_type(4))) unsigned u32x4;
typedef __attribute__((ext_vector_type(2))) unsigned u32x2;
typedef __attribute__((ext_vector_type(2))) __bf16 bf16x2_t;
typedef __attribute__((address_space(3))) s16x4* lds_s16x4_ptr;

#define DI __device__ __forceinline__
#define MFMA16(a, b, c) __builtin_amdgcn_mfma_f32_16x16x32_bf16((a), (b), (c), 0, 0, 0)

#define STAMP(i)
#define STAMPW(i)
#define STAMPEND(i)
namespace {

constexpr int NB = 16, SQ = 96, DM = 512, HQ = 32, DH = 128, HD = HQ * DH;

DI unsigned pack2(float a, float b) {
  f32x2 v = {a, b};
  return __builtin_bit_cast(unsigned, __builtin_convertvector(v, bf16x2_t));
}
DI s16x4 tr_read(unsigned lds_addr) {
  return __builtin_amdgcn_ds_read_tr16_b64_v4i16((lds_s16x4_ptr)(uintptr_t)lds_addr);
}
DI bf16x8 cat8(s16x4 lo, s16x4 hi) { return __builtin_shufflevector(lo, hi, 0, 1, 2, 3, 4, 5, 6, 7); }
DI float xg_max(float x) {
  auto s = __builtin_amdgcn_permlane16_swap(__float_as_uint(x), __float_as_uint(x), false, false);
  x = fmaxf(__uint_as_float(s[0]), __uint_as_float(s[1]));
  auto t = __builtin_amdgcn_permlane32_swap(__float_as_uint(x), __float_as_uint(x), false, false);
  return fmaxf(__uint_as_float(t[0]), __uint_as_float(t[1]));
}
DI float xg_sum(float x) {
  auto s = __builtin_amdgcn_permlane16_swap(__float_as_uint(x), __float_as_uint(x), false, false);
  x = __uint_as_float(s[0]) + __uint_as_float(s[1]);
  auto t = __builtin_amdgcn_permlane32_swap(__float_as_uint(x), __float_as_uint(x), false, false);
  return __uint_as_float(t[0]) + __uint_as_float(t[1]);
}

constexpr int K1_LDS = 160 * 1024;
constexpr int XBASE = 96 * 1024;

constexpr int piece_blk(int i) {
  constexpr int tb[24] = {15, 16, 17, 18, 19, 4, 5, 6, 7, 12, 8, 9,
                          15, 16, 17, 18, 10, 11, 2, 3, 0, 1, 13, 14};
  return tb[i];
}
constexpr unsigned blk_tab(int kind, int pc) {
  unsigned r = 0;
  for (int u = 0; u < 4; ++u) r |= (unsigned)piece_blk(kind * 12 + pc * 4 + u) << (8 * u);
  return r;
}
constexpr int st_blk(int role, int st, int i) {
  constexpr int f0[3][2] = {{19, 4}, {7, 12}, {5, 6}};
  constexpr int r0[2][4] = {{13, 14, 5, 6}, {15, 16, 10, 11}}, r1[2][4] = {{8, 9, 19, 4}, {17, 18, 2, 3}};
  if (role == 0) return st < 3 ? f0[st][i] : r0[((st - 1) >> 1) & 1][((st - 1) & 1) * 2 + i];
  return r1[(st >> 1) & 1][(st & 1) * 2 + i];
}
constexpr unsigned EMPTY = 0x7f817f81u;
constexpr unsigned WS_LINES = 32u << 20;
constexpr int SPIN_CAP = 1 << 16;

constexpr int n_w(int s) { return (s + 3 <= 15) ? 8 : 0; }
constexpr int n_x(int s) { return ((s & 1) && ((s + 3) / 2 <= 7)) ? 3 : 0; }
constexpr int n0(int s) { return n_w(s) + (s >= 13 ? 4 : 0); }
constexpr int cnt0(int s) {
  int n = 11;
  for (int j = 0; j <= s; ++j) n += n0(j);
  return n;
}
constexpr int pos_w(int st) { return st <= 2 ? 11 : cnt0(st - 4) + 8; }
constexpr int cmax(int a, int b) { return a > b ? a : b; }
constexpr int wait0(int s) { return cnt0(s - 1) - pos_w(s + 1); }
constexpr int n1(int s) {
  return n_x(s) + (s >= 1 && s <= 12 ? 2 : 0) + (s <= 9 ? 1 : 0) + (s == 13 ? 4 : s == 15 ? 8 : 0) + (s == 2 ? 3 : s == 4 ? 4 : s == 6);
}
constexpr int cnt1(int s) {
  int n = 21;
  for (int j = 0; j <= s; ++j) n += n1(j);
  return n;
}
constexpr int pos_k(int i) { return cnt1(i) + n_x(i + 1) + 2; }
constexpr int wait1(int s) {
  int need = ((s & 1) && s >= 3 && s < 15) ? cnt1(s - 3) + 3 : s == 1 ? 20 : 0;
  if (s == 13) need = cmax(need, pos_k(9));
  if (s == 14) need = cmax(need, pos_k(11));
  return need ? cnt1(s - 1) - need : -1;
}

template <int N> DI void wait_vm() { asm volatile("s_waitcnt vmcnt(%0)" ::"n"(N) : "memory"); }
DI void raw_barrier() {
  asm volatile("s_waitcnt lgkmcnt(0)" ::: "memory");
  __builtin_amdgcn_s_barrier();
  asm volatile("" ::: "memory");
}
DI void dma16(unsigned voff, const void* sbase, unsigned lds_dst) {
  unsigned keep;
  asm volatile("s_mov_b32 %0, m0\n\ts_mov_b32 m0, %3\n\ts_nop 0\n\tglobal_load_lds_dwordx4 %1, %2\n\ts_mov_b32 m0, %0"
               : "=&s"(keep) : "v"(voff), "s"(sbase), "s"(lds_dst) : "memory");
}
DI void dma16_nt(unsigned voff, const void* sbase, unsigned lds_dst) {
  unsigned keep;
  asm volatile("s_mov_b32 %0, m0\n\ts_mov_b32 m0, %3\n\ts_nop 0\n\tglobal_load_lds_dwordx4 %1, %2 nt\n\ts_mov_b32 m0, %0"
               : "=&s"(keep) : "v"(voff), "s"(sbase), "s"(lds_dst) : "memory");
}
template <int LO, int HI> DI void touch(const char* a) {
  asm volatile("s_mov_b32 exec_lo, %1\n\ts_mov_b32 exec_hi, %2\n\tglobal_load_dword v255, %0, off\n\ts_mov_b64 exec, -1" ::"v"(a), "n"(LO), "n"(HI) : "v255", "memory");
}
DI void dma16_48(unsigned voff, const void* sbase, unsigned lds_dst) {
  unsigned keep;
  asm volatile("s_mov_b32 %0, m0\n\ts_mov_b32 m0, %3\n\ts_mov_b32 exec_hi, 0xffff\n\tglobal_load_lds_dwordx4 %1, %2\n\ts_mov_b32 exec_hi, -1\n\ts_mov_b32 m0, %0"
               : "=&s"(keep) : "v"(voff), "s"(sbase), "s"(lds_dst) : "memory");
}
DI void gload_nt(u32x4& d, unsigned voff, const void* sbase) {
  asm volatile("global_load_dwordx4 %0, %1, %2 nt" : "=v"(d) : "v"(voff), "s"(sbase) : "memory");
}
DI void mfma_acc(f32x4& c, bf16x8 a, bf16x8 b) {
  asm volatile("v_mfma_f32_16x16x32_bf16 %0, %1, %2, %0" : "+v"(c) : "v"(a), "v"(b));
}
DI void mfma_init(f32x4& c, bf16x8 a, bf16x8 b) {
  asm volatile("v_mfma_f32_16x16x32_bf16 %0, %1, %2, 0" : "=&v"(c) : "v"(a), "v"(b));
}
DI int lane_id() {
  int l;
  asm volatile("v_mbcnt_lo_u32_b32 %0, -1, 0\n\tv_mbcnt_hi_u32_b32 %0, -1, %0" : "=v"(l));
  return l;
}
template <int V> struct IC { static constexpr int value = V; };
typedef const __attribute__((address_space(3))) bf16x8* lds_frag_ptr;
typedef __attribute__((address_space(3))) u32x4* lds_u32x4_ptr;
DI bf16x8 lds_frag(unsigned a) { return *(lds_frag_ptr)(uintptr_t)a; }

__global__ __launch_bounds__(512, 2) void fused_kernel(
    const short* __restrict__ xA, const short* __restrict__ kA, const short* __restrict__ vA,
    const short* __restrict__ wqA, const short* __restrict__ woA, const short* __restrict__ xB,
    const short* __restrict__ kB, const short* __restrict__ vB, const short* __restrict__ wqB,
    const short* __restrict__ woB, char* p_ws, unsigned short* __restrict__ out) {
  extern __shared__ __attribute__((aligned(16))) char smem[];
  const int wave = __builtin_amdgcn_readfirstlane(threadIdx.x >> 6);
  const int bid = blockIdx.x;
  const int xcd = bid & 7, idx = bid >> 3;
  const int blk = xcd >> 2;
  const int hg = (xcd & 3) * 2 + (idx & 1);
  const int b = idx >> 1;
  const int h0 = hg * 4;
  const int u = wave >> 1, qh = wave & 1;

  const char* __restrict__ x = (const char*)(blk ? xB : xA) + (size_t)b * SQ * DM * 2;
  const char* __restrict__ kc = (const char*)(blk ? kB : kA) + ((size_t)b * HQ + h0) * (SQ * DH * 2);
  const char* __restrict__ vc = (const char*)(blk ? vB : vA) + ((size_t)b * HQ + h0) * (SQ * DH * 2);
  const char* __restrict__ wq = (const char*)(blk ? wqB : wqA) + h0 * DH * 2;
  const char* __restrict__ wo = (const char*)(blk ? woB : woA) + (size_t)h0 * DH * DM * 2;

  const unsigned lds0 = (unsigned)(uintptr_t)smem;
  STAMP(0);

  const int w4 = wave & 3;
  const int grp = blk * NB + b;
  const char* lines = p_ws + WS_LINES + (size_t)(grp * 32 + w4) * 128;
  const char* src = p_ws + (size_t)(((grp * 8 + hg) * 8 * 4 + w4) * 3072);
  auto pc_of = [&](int p) { return (hg + 1 + p) & 7; };
  auto to_mask = [&](int n) { return ((((1u << n) - 1u) * 0x101u) >> ((7 - hg) & 7)) & 0xffu; };
  auto from_mask = [&](int n) { return ((((1u << n) - 1u) * 0x101u) >> ((n - hg) & 7)) & 0xffu; };
  auto front = [&](auto rc) __attribute__((always_inline)) {
    constexpr int ROLE = decltype(rc)::value;
    const int lane = lane_id();
    const int c = lane & 15, g = lane >> 4, qp = (lane & 15) >> 2, p = lane & 3;
    unsigned vw[2], vx[2], vk[2], vv[2];
#pragma unroll
    for (int e = 0; e < 2; ++e) {
      const int f = g | (e << 2);
      const int ch = (((c >> 1) ^ f) << 1) | (c & 1);
      vw[e] = (unsigned)(g * (HD * 2) + w4 * (DH * 2) + ch * 16);
      const int r = 24 * w4 + 8 * e + (lane >> 3);
      vx[e] = (unsigned)((24 * w4 + (lane >> 3)) * (DM * 2) + (((lane & 7) ^ ((r >> 1) & 7)) << 4));
      const int row = 8 * w4 + 4 * e + g;
      vk[e] = (unsigned)(row * 256 + ((c ^ (row & 15)) << 4));
      vv[e] = (unsigned)(row * 256 + (((((c >> 1) ^ (row & 7)) << 1) | (c & 1)) << 4));
    }

    auto issue_w = [&](int st, int e) {
      dma16(vw[(e >> 1) & 1], wq + (size_t)(st * 32 + 4 * e) * (HD * 2), lds0 + (st % 3) * 32768 + (w4 * 8 + e) * 1024);
    };
    auto issue_x = [&](int j, int e) {
      dma16(vx[e & 1], x + j * 128 + e * 8 * (DM * 2), lds0 + XBASE + (j & 1) * 12288 + (24 * w4 + 8 * e) * 128);
    };
    auto piece_src = [&](int i) { return (i >= 12 ? vc : kc) + (i & 3) * (SQ * DH * 2) + ((i % 12) >> 2) * 8192; };
    auto piece_dst = [&](int i, int e) { return lds0 + piece_blk(i) * 8192 + (w4 * 2 + e) * 1024; };
    auto dma_piece = [&](int i, int e) { dma16_nt(i >= 12 ? vv[e] : vk[e], piece_src(i), piece_dst(i, e)); };

    const int fl = qp | ((g & 1) << 2);
    unsigned trw[8];
#pragma unroll
    for (int dt = 0; dt < 8; ++dt) trw[dt] = lds0 + u * 8192 + (8 * g + qp) * 256 + 8 * p + ((dt ^ fl) << 5);
    unsigned xrd[2];
#pragma unroll
    for (int hf = 0; hf < 2; ++hf)
      xrd[hf] = lds0 + XBASE + (qh * 48 + c) * 128 + (((4 * hf + g) ^ ((c >> 1) & 7)) << 4);
    auto read_a = [&](int st, int dt) {
      const unsigned a0 = trw[dt] + (st % 3) * 32768;
      return cat8(tr_read(a0), tr_read(a0 + 4 * 256));
    };
    auto read_x = [&](int st, int t) { return lds_frag(xrd[st & 1] + ((st >> 1) & 1) * 12288 + t * 16 * 128); };

    f32x4 qacc[8][3];
    bf16x8 af[8], xf[3];
    u32x4 kst[14], vst[4];
    unsigned seen = 0, nonce = 0;

    const int tl = lane * 4 + w4;
    const char* ta = wq + (size_t)((tl >> 4) * 32 + 2 * b + ((tl >> 3) & 1)) * (HD * 2) + (tl & 7) * 128;
    if constexpr (ROLE == 0) {
#pragma unroll
      for (int e = 0; e < 3; ++e) issue_x(0, e);
#pragma unroll
      for (int e = 0; e < 8; ++e) issue_w(0, e);
      wait_vm<0>();
    } else {
      touch<0xffff0, 0>(ta);
      wait_vm<0>();
#pragma unroll
      for (int e = 8; e < 24; ++e) issue_w(e >> 3, e & 7);
#pragma unroll
      for (int e = 0; e < 3; ++e) issue_x(1, e);
      touch<0xf00000, 0>(ta);
    }
    raw_barrier();
    STAMP(1);
    STAMPW(2);
#pragma unroll
    for (int t = 0; t < 3; ++t) xf[t] = read_x(0, t);
#pragma unroll
    for (int dt = 0; dt < 8; ++dt) af[dt] = read_a(0, dt);

    auto mem_op = [&](auto sc, auto kc_) {
      constexpr int s = decltype(sc)::value, k = decltype(kc_)::value;
      if constexpr (ROLE == 0) {
        if constexpr (k < n_w(s)) issue_w(s + 3, k);
        else if constexpr (s == 13 && k < 4) gload_nt(vst[k], vv[k & 1], piece_src(12 + (k >> 1)));
        else if constexpr (s == 14 && k < 4) dma_piece(16 + (k >> 1), k & 1);
        else if constexpr (s == 15 && k < 4) dma_piece(20 + (k >> 1), k & 1);
      } else {
        constexpr int k2 = k - n_x(s);
        if constexpr (k2 < 0) {
          issue_x((s + 3) / 2, k);
        } else if constexpr (s >= 1 && s <= 12 && k2 < 2) {
          if constexpr (s - 1 < 5) dma_piece(s - 1, k2);
          else gload_nt(kst[(s - 6) * 2 + k2], vk[k2], piece_src(s - 1));
        } else if constexpr (s <= 9 && k2 == (s == 0 ? 0 : 2)) {
          constexpr unsigned long long m = 15ull << (4 * (s + 6));
          touch<(int)(unsigned)m, (int)(unsigned)(m >> 32)>(ta);
        } else if constexpr (s == 2 && k2 == 3) {
          asm volatile("s_mov_b64 exec, 0xff\n\tglobal_load_dword %0, %1, %2 sc1\n\ts_mov_b64 exec, -1"
                       : "=v"(seen) : "v"(lane * 16), "s"(lines + hg * 512) : "memory");
        } else if constexpr ((s == 2 && k2 >= 4 && k2 < 6) || (s == 4 && k2 >= 3 && k2 < 7)) {
          constexpr int j = s == 2 ? k2 - 4 : k2 - 1;
          asm volatile("global_store_dwordx4 %0, %1, %2 offset:%3 sc1\n\ts_nop 1" ::"v"(lane * 16), "v"((u32x4){EMPTY, EMPTY, EMPTY, EMPTY}),
                       "s"(src + (6 + j / 3) * 12288), "n"((j % 3) * 1024) : "memory");
        } else if constexpr (s == 6 && k2 == 3) {
          asm volatile("" : "+v"(seen));
          unsigned used = 0;
#pragma unroll
          for (int i = 0; i < 8; ++i) {
            const unsigned w = __builtin_amdgcn_readlane(seen, i);
            used |= w < 16u ? 1u << w : 0u;
          }
          nonce = __builtin_ctz(~used);
          asm volatile("s_mov_b64 exec, 1\n\tglobal_store_dword %0, %1, %2 sc1\n\ts_mov_b64 exec, -1"
                       ::"v"(lane * 16), "v"(nonce), "s"(lines + hg * 528) : "memory");
        } else if constexpr (s == 13 && k < 10) {
          *(lds_u32x4_ptr)(uintptr_t)(piece_dst(5 + (k >> 1), k & 1) + lane * 16) = kst[k];
        } else if constexpr (s == 13 && k < 14) {
          gload_nt(vst[k - 10], vv[k & 1], piece_src(14 + ((k - 10) >> 1)));
        } else if constexpr (s == 14 && k < 4) {
          *(lds_u32x4_ptr)(uintptr_t)(piece_dst(10 + (k >> 1), k & 1) + lane * 16) = kst[10 + k];
        } else if constexpr (s == 15 && k < 8) {
          dma_piece(18 + (k >> 1) + (k >= 4 ? 2 : 0), k & 1);
        }
      }
    };
    auto group = [&](auto sc, auto dc, bf16x8* an, bf16x8* xn) {
      constexpr int s = decltype(sc)::value, dt = decltype(dc)::value;
      constexpr int n = ROLE == 0 ? n0(s) : (s == 13 ? 14 : s == 14 ? 4 : n1(s));
      constexpr int per = n <= 8 ? 1 : n <= 16 ? 2 : 3;
#pragma unroll
      for (int t = 0; t < 3; ++t) {
        if constexpr (s == 0) mfma_init(qacc[dt][t], af[dt], xf[t]);
        else mfma_acc(qacc[dt][t], af[dt], xf[t]);
      }
      if constexpr (s > 0 && s < 15) {
        if constexpr (dt == 0) { an[0] = read_a(s + 1, 0); an[1] = read_a(s + 1, 1); }
        if constexpr (dt >= 1 && dt <= 3) xn[dt - 1] = read_x(s + 1, dt - 1);
        if constexpr (dt >= 1 && dt < 7) an[dt + 1] = read_a(s + 1, dt + 1);
      }
      mem_op(sc, IC<dt * per>{});
      if constexpr (per > 1) mem_op(sc, IC<dt * per + 1>{});
      if constexpr (per > 2) mem_op(sc, IC<dt * per + 2>{});
      __builtin_amdgcn_sched_barrier(0);
    };
    auto step = [&](auto ic) {
      constexpr int s = decltype(ic)::value;
      if constexpr (ROLE == 0 && s > 1 && s < 15) wait_vm<wait0(s)>();
      if constexpr (ROLE == 1 && wait1(s) >= 0) wait_vm<wait1(s)>();
      raw_barrier();
      STAMP(3 + s);
      bf16x8 xn[3], an[8];
      group(ic, IC<0>{}, an, xn); group(ic, IC<1>{}, an, xn); group(ic, IC<2>{}, an, xn); group(ic, IC<3>{}, an, xn);
      group(ic, IC<4>{}, an, xn); group(ic, IC<5>{}, an, xn); group(ic, IC<6>{}, an, xn); group(ic, IC<7>{}, an, xn);
      if constexpr (s == 0) {
        if constexpr (ROLE == 1) wait_vm<cnt1(0) - 9>();
        raw_barrier();
#pragma unroll
        for (int t = 0; t < 3; ++t) xn[t] = read_x(1, t);
#pragma unroll
        for (int dt = 0; dt < 8; ++dt) an[dt] = read_a(1, dt);
      }
      if constexpr (s < 15) {
#pragma unroll
        for (int t = 0; t < 3; ++t) xf[t] = xn[t];
#pragma unroll
        for (int dt = 0; dt < 8; ++dt) af[dt] = an[dt];
      }
    };
    step(IC<0>{}); step(IC<1>{}); step(IC<2>{}); step(IC<3>{}); step(IC<4>{}); step(IC<5>{}); step(IC<6>{}); step(IC<7>{});
    step(IC<8>{}); step(IC<9>{}); step(IC<10>{}); step(IC<11>{}); step(IC<12>{}); step(IC<13>{}); step(IC<14>{}); step(IC<15>{});

    asm volatile("s_nop 7\n\ts_nop 7" ::: "memory");
    __builtin_amdgcn_sched_barrier(0);
    STAMPW(19);
    bf16x8 qf[4][3];
#pragma unroll
    for (int s = 0; s < 4; ++s)
#pragma unroll
      for (int t = 0; t < 3; ++t) {
        u32x4 r;
#pragma unroll
        for (int e = 0; e < 2; ++e) {
          const unsigned a = pack2(qacc[2 * s][t][2 * e], qacc[2 * s][t][2 * e + 1]);
          const unsigned bq = pack2(qacc[2 * s + 1][t][2 * e], qacc[2 * s + 1][t][2 * e + 1]);
          auto r1 = __builtin_amdgcn_permlane32_swap(a, bq, false, false);
          auto r2 = __builtin_amdgcn_permlane16_swap(r1[0], r1[1], false, false);
          r[e] = r2[0];
          r[2 + e] = r2[1];
        }
        qf[s][t] = __builtin_bit_cast(bf16x8, r);
      }
    STAMPW(20);
    const int lane1 = lane_id();
    const int c1 = lane1 & 15, g1 = lane1 >> 4, qp1 = (lane1 & 15) >> 2, p1 = lane1 & 3;
    const int fl1 = qp1 | ((g1 & 1) << 2);
    if constexpr (ROLE == 1) raw_barrier();

    f32x4 sacc[6][3];
#pragma unroll
    for (int kt = 0; kt < 6; ++kt)
#pragma unroll
      for (int t = 0; t < 3; ++t) sacc[kt][t] = (f32x4){0.f, 0.f, 0.f, 0.f};
#pragma unroll
    for (int pc = 0; pc < 3; ++pc) {
      const unsigned kblk = (blk_tab(0, pc) >> (8 * u)) & 255u;
      const unsigned kb = lds0 + kblk * 8192 + c1 * 256;
#pragma unroll
      for (int k2 = 0; k2 < 2; ++k2) {
#pragma unroll
        for (int s = 0; s < 4; ++s) {
          const bf16x8 kf = lds_frag(kb + k2 * 4096 + (((4 * s + g1) ^ c1) << 4));
#pragma unroll
          for (int t = 0; t < 3; ++t) sacc[pc * 2 + k2][t] = MFMA16(kf, qf[s][t], sacc[pc * 2 + k2][t]);
        }
      }
    }
    STAMPW(21);
    wait_vm<8>();
    raw_barrier();
#pragma unroll
    for (int k = 0; k < 4; ++k)
      *(lds_u32x4_ptr)(uintptr_t)(piece_dst(12 + 2 * ROLE + (k >> 1), k & 1) + lane1 * 16) = vst[k];
    __builtin_amdgcn_sched_barrier(0);

    unsigned vo;
    {
      const int srow = lane1 >> 3, pos = lane1 & 7;
      vo = (unsigned)((w4 * 32 + srow) * (DM * 2) + (((((pos >> 1) ^ ((srow >> 1) & 3)) << 1) | (pos & 1)) << 4));
    }
    auto issue_wo = [&](int role, int st, int e) {
      const int pc = pc_of(st >> 1);
      const unsigned dst = lds0 + (unsigned)((w4 >> 1) ? st_blk(role, st, 1) : st_blk(role, st, 0)) * 8192 + (w4 & 1) * 4096 + e * 1024;
      dma16(vo, wo + (size_t)((role * 2 + (st & 1)) * 128 + e * 8) * (DM * 2) + pc * 128, dst);
    };
#pragma unroll
    for (int e = 0; e < 8; ++e) {
      if constexpr (ROLE == 0) issue_wo(0, e >> 2, e & 3);
      else issue_wo(e >> 2, e < 4 ? 2 : 0, e & 3);
    }
    {
      const int tw = (lane1 & 31) * 8 + wave;
      touch<-1, 0>(wo + ((tw >> 4) * 32 + 2 * b + ((tw >> 3) & 1)) * (DM * 2) + (tw & 7) * 128);
    }

    const float cexp = 0.08838834764831845f * 1.4426950408889634f;
    bf16x8 pf[3][3];
    float rinv[3];
#pragma unroll
    for (int t = 0; t < 3; ++t) {
      float m = sacc[0][t][0];
#pragma unroll
      for (int kt = 0; kt < 6; ++kt)
#pragma unroll
        for (int r = 0; r < 4; ++r) m = fmaxf(m, sacc[kt][t][r]);
      m = xg_max(m);
      const float mc = m * cexp;
      float sum = 0.f;
#pragma unroll
      for (int kt = 0; kt < 6; ++kt)
#pragma unroll
        for (int r = 0; r < 4; ++r) {
          const float e = __builtin_amdgcn_exp2f(fmaf(sacc[kt][t][r], cexp, -mc));
          sacc[kt][t][r] = e;
          sum += e;
        }
      sum = xg_sum(sum);
      rinv[t] = 1.0f / sum;
#pragma unroll
      for (int s = 0; s < 3; ++s) {
        u32x4 r;
        r[0] = pack2(sacc[2 * s][t][0], sacc[2 * s][t][1]);
        r[1] = pack2(sacc[2 * s][t][2], sacc[2 * s][t][3]);
        r[2] = pack2(sacc[2 * s + 1][t][0], sacc[2 * s + 1][t][1]);
        r[3] = pack2(sacc[2 * s + 1][t][2], sacc[2 * s + 1][t][3]);
        pf[s][t] = __builtin_bit_cast(bf16x8, r);
      }
    }
    STAMPW(22);
    wait_vm<9>();
    raw_barrier();

    f32x4 oacc[8][3];
#pragma unroll
    for (int dt = 0; dt < 8; ++dt)
#pragma unroll
      for (int t = 0; t < 3; ++t) oacc[dt][t] = (f32x4){0.f, 0.f, 0.f, 0.f};
    const unsigned vlane = lds0 + (4 * g1 + qp1) * 256 + 8 * p1;
#pragma unroll
    for (int s = 0; s < 3; ++s) {
      const unsigned vblk = (blk_tab(1, s) >> (8 * u)) & 255u;
      const unsigned vb = vlane + vblk * 8192;
#pragma unroll
      for (int dt = 0; dt < 8; ++dt) {
        const unsigned a0 = vb + ((dt ^ fl1) << 5);
        const bf16x8 vf = cat8(tr_read(a0), tr_read(a0 + 16 * 256));
#pragma unroll
        for (int t = 0; t < 3; ++t) oacc[dt][t] = MFMA16(vf, pf[s][t], oacc[dt][t]);
      }
    }
    STAMPW(23);
    if constexpr (ROLE == 0) wait_vm<5>();
    raw_barrier();
    if constexpr (ROLE == 1) {
#pragma unroll
      for (int e = 0; e < 4; ++e) issue_wo(1, 1, e);
    }

#pragma unroll
    for (int s = 0; s < 4; ++s)
#pragma unroll
      for (int t = 0; t < 3; ++t) {
        const float ri = rinv[t];
        u32x4 r;
        r[0] = pack2(oacc[2 * s][t][0] * ri, oacc[2 * s][t][1] * ri);
        r[1] = pack2(oacc[2 * s][t][2] * ri, oacc[2 * s][t][3] * ri);
        r[2] = pack2(oacc[2 * s + 1][t][0] * ri, oacc[2 * s + 1][t][1] * ri);
        r[3] = pack2(oacc[2 * s + 1][t][2] * ri, oacc[2 * s + 1][t][3] * ri);
        const int f = s * 6 + qh * 3 + t;
        const unsigned tab = (f >> 3) == 0 ? blk_tab(1, 0) : (f >> 3) == 1 ? blk_tab(1, 1) : blk_tab(1, 2);
        const unsigned a = lds0 + ((tab >> (8 * u)) & 255u) * 8192 + (f & 7) * 1024 + lane1 * 16;
        *(lds_u32x4_ptr)(uintptr_t)a = r;
      }
    const unsigned l16 = (unsigned)lane1 * 16;
    const unsigned wlane = lds0 + (4 * g1 + qp1) * 128 + 8 * p1 + ((w4 ^ ((2 * g1 + (qp1 >> 1)) & 3)) << 5);
    unsigned wl[3] = {wlane, wlane + 65536, wlane + 131072};
    asm volatile("" : "+v"(wl[0]), "+v"(wl[1]), "+v"(wl[2]));
    auto read_wo = [&](int st, int ks) {
      const int bk = st_blk(ROLE, st, ks >> 1);
      const unsigned a0 = wl[bk >> 3] + (unsigned)((bk & 7) * 8192 + (ks & 1) * 4096);
      return cat8(tr_read(a0), tr_read(a0 + 16 * 128));
    };
    auto read_o = [&](int kk, int t) {
      const int f = (kk & 3) * 6 + t;
      return lds_frag(lds0 + l16 + piece_blk(12 + (f >> 3) * 4 + ROLE * 2 + (kk >> 2)) * 8192 + (f & 7) * 1024);
    };
    const unsigned hand = lds0 + ((0x0c070100u >> (8 * w4)) & 255u) * 8192 + l16;
    f32x4 acc[6];
    bf16x8 wf[4], of[8][6];
    unsigned tag = 0;
    if constexpr (ROLE == 0) {
#pragma unroll
      for (int ks = 0; ks < 4; ++ks) wf[ks] = read_wo(0, ks);
      wait_vm<0>();
      raw_barrier();
      STAMP(24);
    }
    auto flag_out = [&](unsigned m) {
      unsigned a;
      asm volatile("v_lshlrev_b32 %0, 5, %1\n\ts_mov_b32 exec_lo, %4\n\ts_mov_b32 exec_hi, 0\n\tglobal_store_dword %0, %2, %3 sc1\n\ts_mov_b64 exec, -1"
                   : "=&v"(a) : "v"(l16), "v"(tag), "s"(lines + hg * 16), "s"(m) : "memory");
    };
    auto hand_put = [&]() {
      asm volatile("s_nop 3" ::: "memory");
#pragma unroll
      for (int t = 0; t < 6; ++t) {
        *(lds_u32x4_ptr)(uintptr_t)(hand + t * 1024) = __builtin_bit_cast(u32x4, acc[t]);
        __builtin_amdgcn_sched_barrier(0);
      }
    };
    auto hand_get = [&](int t) { return __builtin_bit_cast(f32x4, *(lds_u32x4_ptr)(uintptr_t)(hand + t * 1024)); };
    auto add_in = [&](u32x4 (&in)[3]) {
#pragma unroll
      for (int j = 0; j < 3; ++j) {
        asm volatile("" : "+v"(in[j]));
#pragma unroll
        for (int e = 0; e < 4; ++e) {
          acc[2 * j + (e >> 1)][2 * (e & 1)] += __uint_as_float(in[j][e] << 16);
          acc[2 * j + (e >> 1)][2 * (e & 1) + 1] += __uint_as_float(in[j][e] & 0xffff0000u);
        }
        asm volatile("" : "+v"(acc[2 * j]), "+v"(acc[2 * j + 1]));
      }
      __builtin_amdgcn_sched_barrier(0);
    };
    auto load_in = [&](u32x4 (&in)[3], int k) {
#pragma unroll
      for (int j = 0; j < 3; ++j)
        asm volatile("global_load_dwordx4 %0, %1, %2 offset:%3 sc1" : "=v"(in[j]) : "v"(l16), "s"(src + k * 12288), "n"(j * 1024) : "memory");
    };
    auto poll = [&](unsigned& f) {
      asm volatile("s_mov_b64 exec, 0xff\n\tglobal_load_dword %0, %1, %2 sc1\n\ts_mov_b64 exec, -1"
                   : "=v"(f) : "v"(l16), "s"(lines + hg * 512) : "memory");
    };
    auto marks = [&](unsigned& m) {
      unsigned a;
      asm volatile("v_mul_u32_u24 %1, 33, %2\n\ts_mov_b64 exec, 0xff\n\tglobal_load_dword %0, %1, %3 sc1\n\ts_mov_b64 exec, -1"
                   : "+v"(m), "=&v"(a) : "v"(l16), "s"(lines) : "memory");
    };
    auto ready = [&](unsigned f, unsigned need) {
      asm volatile("" : "+v"(f));
      const unsigned got = (unsigned)__builtin_amdgcn_ballot_w64(f == __builtin_amdgcn_readlane(f, hg));
      return (got & need) == need;
    };
    auto retag = [&](unsigned m, unsigned sent) {
      asm volatile("" : "+v"(m));
      const unsigned bad = (unsigned)__builtin_amdgcn_ballot_w64(m != tag) & sent;
      tag = m;
      if (bad) flag_out(bad);
    };
    unsigned pf0 = 0, pf1 = 0, early = 0, e5 = 0;
    u32x4 ein[5][3];
    auto oint = [&](auto nc) {
      constexpr int n = decltype(nc)::value;
      constexpr int h = ROLE == 0 ? n : n - 2;
      constexpr bool second = n >= 2 && ((n & 1) != ROLE);
      constexpr int la = (second || (ROLE == 0 && n < 2)) && h + 1 <= 15 ? h + 1 : -1;
      constexpr int nst = (second || (ROLE == 0 && n == 1)) ? (h + 2 <= 15) + (h + 3 <= 15) : 0;
      constexpr bool fin = ROLE == 1 && h >= 0 && (h & 1) && h < 15;
      constexpr bool cin = ROLE == 1 && h >= 0 && !(h & 1) && h < 14;
      if constexpr (second) {
        if constexpr (cin) {
#pragma unroll
          for (int t = 0; t < 6; ++t) acc[t] = hand_get(t);
        }
#pragma unroll
        for (int ks = 0; ks < 4; ++ks) wf[ks] = read_wo(h, ks);
        if constexpr (ROLE == 1 && n == 12) poll(pf0);
#pragma unroll
        for (int j = 0; j < 4 * nst; ++j) issue_wo(ROLE, h + 2 + (j >> 2), j & 3);
        if constexpr (ROLE == 0 && n == 15) {
          asm volatile("s_cmp_lg_u32 %13, 0\n\ts_cbranch_scc1 2f\n\ts_waitcnt vmcnt(0)\n\ts_branch 1f\n2:\n\t"
                       "global_load_dwordx4 %0, %12, %14 sc1\n\tglobal_load_dwordx4 %1, %12, %14 offset:1024 sc1\n\tglobal_load_dwordx4 %2, %12, %14 offset:2048 sc1\n\t"
                       "global_load_dwordx4 %3, %12, %15 sc1\n\tglobal_load_dwordx4 %4, %12, %15 offset:1024 sc1\n\tglobal_load_dwordx4 %5, %12, %15 offset:2048 sc1\n\t"
                       "global_load_dwordx4 %6, %12, %16 sc1\n\tglobal_load_dwordx4 %7, %12, %16 offset:1024 sc1\n\tglobal_load_dwordx4 %8, %12, %16 offset:2048 sc1\n\t"
                       "global_load_dwordx4 %9, %12, %17 sc1\n\tglobal_load_dwordx4 %10, %12, %17 offset:1024 sc1\n\tglobal_load_dwordx4 %11, %12, %17 offset:2048 sc1\n1:"
                       : "=&v"(ein[0][0]), "=&v"(ein[0][1]), "=&v"(ein[0][2]), "=&v"(ein[1][0]), "=&v"(ein[1][1]), "=&v"(ein[1][2]),
                         "=&v"(ein[2][0]), "=&v"(ein[2][1]), "=&v"(ein[2][2]), "=&v"(ein[3][0]), "=&v"(ein[3][1]), "=&v"(ein[3][2])
                       : "v"(l16), "s"(early), "s"(src + 12288), "s"(src + 2 * 12288), "s"(src + 3 * 12288), "s"(src + 4 * 12288)
                       : "memory", "scc");
        }
        __builtin_amdgcn_sched_barrier(0);
      }
      if constexpr (ROLE == 0 && n == 0) {
#pragma unroll
        for (int i = 0; i < 12; ++i) of[i / 6][i % 6] = read_o(i / 6, i % 6);
        __builtin_amdgcn_sched_barrier(0);
      }
      if constexpr (ROLE == 1 && n == 1) {
        marks(tag);
#pragma unroll
        for (int i = 0; i < 48; ++i) of[i / 6][i % 6] = read_o(i / 6, i % 6);
      }
#pragma unroll
      for (int i = 0; i < 24; ++i) {
        const int ks = i / 6, t = i % 6;
        if constexpr (h >= 0) {
          if (!(h & 1) && !cin && ks == 0) mfma_init(acc[t], wf[ks], of[(h & 1) * 4 + ks][t]);
          else mfma_acc(acc[t], wf[ks], of[(h & 1) * 4 + ks][t]);
        }
        if constexpr (ROLE == 0 && n == 0) {
          if (i < 18) {
            of[(12 + 2 * i) / 6][(12 + 2 * i) % 6] = read_o((12 + 2 * i) / 6, (12 + 2 * i) % 6);
            of[(13 + 2 * i) / 6][(13 + 2 * i) % 6] = read_o((13 + 2 * i) / 6, (13 + 2 * i) % 6);
          }
        }
        if constexpr (la >= 0) { if (t == 5) wf[ks] = read_wo(la, ks); }
        if constexpr (ROLE == 0 && n == 1) { if (i % 3 == 0) issue_wo(0, 3 + i / 12, (i / 3) & 3); }
        if constexpr (ROLE == 0 && n == 15) {
          if (i == 11) {
            wait_vm<12>();
            e5 = __builtin_amdgcn_readfirstlane(early && ready(pf1, from_mask(5)) ? 1u : 0u);
            asm volatile("s_cmp_eq_u32 %4, 0\n\ts_cbranch_scc1 1f\n\t"
                         "global_load_dwordx4 %0, %3, %5 sc1\n\tglobal_load_dwordx4 %1, %3, %5 offset:1024 sc1\n\tglobal_load_dwordx4 %2, %3, %5 offset:2048 sc1\n1:"
                         : "=&v"(ein[4][0]), "=&v"(ein[4][1]), "=&v"(ein[4][2]) : "v"(l16), "s"(e5), "s"(src + 5 * 12288) : "memory", "scc");
          }
        }
        __builtin_amdgcn_sched_barrier(0);
      }
      if constexpr (ROLE == 0 && (n & 1) && n < 15) hand_put();
      if constexpr (fin) {
        constexpr int p = h >> 1;
        if constexpr (p == 5) {
          wait_vm<8>();
          if (!ready(pf0, from_mask(2))) {
#pragma unroll 1
            for (int it = 0; it < SPIN_CAP; ++it) {
              unsigned f, m = tag;
              poll(f);
              marks(m);
              wait_vm<0>();
              retag(m, to_mask(4));
              if (ready(f, from_mask(2))) break;
              __builtin_amdgcn_s_sleep(1);
            }
          }
        }
        const char* dst = p_ws + (size_t)((((grp * 8 + pc_of(p)) * 8 + p + 1) * 4 + w4) * 3072);
        asm volatile("s_nop 3" ::: "memory");
        __builtin_amdgcn_sched_barrier(0);
#pragma unroll
        for (int j = 0; j < 3; ++j) {
          const f32x4 a = acc[2 * j], c2 = acc[2 * j + 1];
          u32x4 r;
          r[0] = pack2(a[0], a[1]); r[1] = pack2(a[2], a[3]);
          r[2] = pack2(c2[0], c2[1]); r[3] = pack2(c2[2], c2[3]);
          asm volatile("global_store_dwordx4 %0, %1, %2 offset:%3 sc1\n\ts_nop 1" ::"v"(l16), "v"(r), "s"(dst), "n"(j * 1024) : "memory");
          __builtin_amdgcn_sched_barrier(0);
        }
        wait_vm<3>();
        if constexpr (p >= 1 && p <= 5) flag_out(1u << pc_of(p - 1));
      }
      if constexpr (ROLE == 0 && n == 13) poll(pf0);
      if constexpr (ROLE == 0 && n >= 2 && !(n & 1)) wait_vm<0>();
      if constexpr (ROLE == 0 && n == 14) {
        early = __builtin_amdgcn_readfirstlane(ready(pf0, from_mask(4)) ? 1u : 0u);
        poll(pf1);
      }
      if constexpr (ROLE == 1 && n == 0) wait_vm<4>();
      if constexpr (ROLE == 1 && n == 1) wait_vm<0>();
      if constexpr (n <= 15) raw_barrier();
      if constexpr (ROLE == 1 && n == 0) { STAMP(24); }
      if constexpr ((n & 1) && n <= 15) { STAMP(25 + n / 2); }
    };
    oint(IC<0>{}); oint(IC<1>{}); oint(IC<2>{}); oint(IC<3>{}); oint(IC<4>{}); oint(IC<5>{});
    oint(IC<6>{}); oint(IC<7>{}); oint(IC<8>{}); oint(IC<9>{}); oint(IC<10>{}); oint(IC<11>{});
    oint(IC<12>{}); oint(IC<13>{}); oint(IC<14>{}); oint(IC<15>{});

    if constexpr (ROLE == 0) {
      asm volatile("s_nop 7\n\ts_waitcnt vmcnt(3)\n\ts_cmp_lg_u32 %0, 0\n\ts_cbranch_scc1 1f\n\ts_waitcnt vmcnt(0)\n1:" ::"s"(e5) : "memory", "scc");
      if (early) { add_in(ein[0]); add_in(ein[1]); add_in(ein[2]); add_in(ein[3]); }
      STAMPW(33);
      if (e5) {
        wait_vm<0>();
        add_in(ein[4]);
      } else {
#pragma unroll 1
        for (int it = 0; it < SPIN_CAP; ++it) {
          unsigned f;
          poll(f);
          wait_vm<0>();
          if (ready(f, from_mask(5))) break;
          __builtin_amdgcn_s_sleep(1);
        }
        u32x4 in[3];
#pragma unroll 1
        for (int k = early ? 5 : 1; k <= 5; ++k) {
          load_in(in, k);
          wait_vm<0>();
          add_in(in);
        }
      }
      STAMPW(34);
      hand_put();
      STAMPW(35);
      raw_barrier();
    } else {
      oint(IC<16>{});
      u32x4 in[2][3];
      unsigned f, m = tag;
      load_in(in[0], 6); load_in(in[1], 7);
      poll(f);
      marks(m);
      oint(IC<17>{});
      STAMPW(33);
      auto whole = [&]() {
        bool ok = true;
#pragma unroll
        for (int i = 0; i < 6; ++i) {
          asm volatile("" : "+v"(in[i / 3][i % 3]));
          ok = ok && in[i / 3][i % 3][0] != EMPTY;
        }
        return __builtin_amdgcn_ballot_w64(!ok) == 0;
      };
      wait_vm<0>();
      retag(m, to_mask(5));
      if (!(whole() && ready(f, from_mask(5)))) {
#pragma unroll 1
        for (int it = 0; it < SPIN_CAP; ++it) {
          load_in(in[0], 6); load_in(in[1], 7);
          poll(f);
          m = tag;
          marks(m);
          wait_vm<0>();
          retag(m, to_mask(5));
          if (whole() && ready(f, from_mask(5))) break;
          __builtin_amdgcn_s_sleep(1);
        }
      }
      STAMPW(34);
      m = tag;
      marks(m);
      add_in(in[0]); add_in(in[1]);
      raw_barrier();
#pragma unroll
      for (int t = 0; t < 6; ++t) acc[t] += hand_get(t);
      STAMPW(35);
      const int lane2 = lane_id();
      unsigned short* o = out + (size_t)(b * SQ + (lane2 & 15)) * 1024 + blk * 512 + hg * 64 + w4 * 16 + 4 * (lane2 >> 4);
#pragma unroll
      for (int t = 0; t < 6; ++t) {
        u32x2 r;
        r[0] = pack2(acc[t][0], acc[t][1]);
        r[1] = pack2(acc[t][2], acc[t][3]);
        asm volatile("global_store_dwordx2 %0, %1, off" ::"v"(o + t * 16 * 1024), "v"(r) : "memory");
      }
      wait_vm<6>();
      retag(m, to_mask(5));
    }
    STAMPW(36);
    STAMPEND(37);
  };
  if (wave >= 4) {
    front(IC<1>{});
    __builtin_amdgcn_endpgm();
  }
  front(IC<0>{});
}

}

extern "C" void kernel_launch(void* const* d_in, const int* in_sizes, int n_in, void* d_out, int out_size,
                              void* d_ws, size_t ws_size, hipStream_t stream) {
  const short* xA = (const short*)d_in[0];
  const short* kA = (const short*)d_in[1];
  const short* vA = (const short*)d_in[2];
  const short* wqA = (const short*)d_in[3];
  const short* woA = (const short*)d_in[4];
  const short* xB = (const short*)d_in[5];
  const short* kB = (const short*)d_in[6];
  const short* vB = (const short*)d_in[7];
  const short* wqB = (const short*)d_in[8];
  const short* woB = (const short*)d_in[9];
  (void)hipFuncSetAttribute((const void*)fused_kernel, hipFuncAttributeMaxDynamicSharedMemorySize, K1_LDS);
  hipLaunchKernelGGL(fused_kernel, dim3(256), dim3(512), K1_LDS, stream, xA, kA, vA, wqA, woA, xB, kB, vB, wqB,
                     woB, (char*)d_ws, (unsigned short*)d_out);
}
```

```cpp
#include <hip/hip_runtime.h>
#include <hip/hip_bf16.h>
#include <stdint.h>

typedef __attribute__((ext_vector_type(8))) short bf16x8;
typedef __attribute__((ext_vector_type(4))) short s16x4;
typedef __attribute__((ext_vector_type(4))) float f32x4;
typedef __attribute__((ext_vector_type(2))) float f32x2;
typedef __attribute__((ext_vector_type(4))) unsigned u32x4;
typedef __attribute__((ext_vector_type(2))) unsigned u32x2;
typedef __attribute__((ext_vector_type(2))) __bf16 bf16x2_t;
typedef __attribute__((address_space(3))) s16x4* lds_s16x4_ptr;

#define DI __device__ __forceinline__
#define MFMA16(a, b, c) __builtin_amdgcn_mfma_f32_16x16x32_bf16((a), (b), (c), 0, 0, 0)

#define STAMP(i)
#define STAMPW(i)
#define STAMPEND(i)
namespace {

constexpr int NB = 16, SQ = 96, DM = 512, HQ = 32, DH = 128, HD = HQ * DH;

DI unsigned pack2(float a, float b) {
  f32x2 v = {a, b};
  return __builtin_bit_cast(unsigned, __builtin_convertvector(v, bf16x2_t));
}
DI s16x4 tr_read(unsigned lds_addr) {
  return __builtin_amdgcn_ds_read_tr16_b64_v4i16((lds_s16x4_ptr)(uintptr_t)lds_addr);
}
DI bf16x8 cat8(s16x4 lo, s16x4 hi) { return __builtin_shufflevector(lo, hi, 0, 1, 2, 3, 4, 5, 6, 7); }
DI float xg_max(float x) {
  auto s = __builtin_amdgcn_permlane16_swap(__float_as_uint(x), __float_as_uint(x), false, false);
  x = fmaxf(__uint_as_float(s[0]), __uint_as_float(s[1]));
  auto t = __builtin_amdgcn_permlane32_swap(__float_as_uint(x), __float_as_uint(x), false, false);
  return fmaxf(__uint_as_float(t[0]), __uint_as_float(t[1]));
}
DI float xg_sum(float x) {
  auto s = __builtin_amdgcn_permlane16_swap(__float_as_uint(x), __float_as_uint(x), false, false);
  x = __uint_as_float(s[0]) + __uint_as_float(s[1]);
  auto t = __builtin_amdgcn_permlane32_swap(__float_as_uint(x), __float_as_uint(x), false, false);
  return __uint_as_float(t[0]) + __uint_as_float(t[1]);
}

constexpr int K1_LDS = 160 * 1024;
constexpr int XBASE = 96 * 1024;

constexpr int piece_u(int i) {
  constexpr int t[24] = {0, 1, 2, 3, 0, 1, 2, 3, 0, 1, 2, 3, 0, 1, 2, 3, 3, 0, 0, 1, 2, 3, 1, 2};
  return t[i];
}
constexpr int piece_pc(int i) {
  constexpr int t[24] = {0, 0, 0, 0, 1, 1, 1, 1, 2, 2, 2, 2, 0, 0, 0, 0, 2, 2, 1, 1, 1, 1, 2, 2};
  return t[i];
}
constexpr int piece_blk(int i) {
  const int pc = piece_pc(i);
  return (i < 12 ? (pc == 0 ? 16 : pc == 1 ? 4 : 8) : (pc == 0 ? 8 : pc == 1 ? 0 : 12)) + piece_u(i);
}
constexpr unsigned o_reg(int t) { return t == 0 ? 0u : t == 1 ? 65536u : 98304u; }
constexpr unsigned st_reg(int st) {
  constexpr unsigned r[5] = {32768u, 131072u, 0u, 65536u, 98304u};
  return r[st % 5];
}
constexpr unsigned EMPTY = 0x7f817f81u;
constexpr unsigned WS_LINES = 32u << 20;
constexpr int SPIN_CAP = 1 << 16;

constexpr int n_w(int s) { return (s + 3 <= 15) ? 8 : 0; }
constexpr int n_x(int s) { return ((s & 1) && ((s + 3) / 2 <= 7)) ? 3 : 0; }
constexpr int n0(int s) { return n_w(s) + (s >= 13 ? 4 : 0); }
constexpr int cnt0(int s) {
  int n = 11;
  for (int j = 0; j <= s; ++j) n += n0(j);
  return n;
}
constexpr int pos_w(int st) { return st <= 2 ? 11 : cnt0(st - 4) + 8; }
constexpr int cmax(int a, int b) { return a > b ? a : b; }
constexpr int wait0(int s) { return cnt0(s - 1) - pos_w(s + 1); }
constexpr int n1(int s) {
  return n_x(s) + (s >= 1 && s <= 12 ? 2 : 0) + (s <= 8 ? 1 : 0) + (s == 13 ? 4 : s == 15 ? 8 : 0) + (s == 2 ? 3 : s == 4 ? 4 : s == 8 ? 3 : s == 6);
}
constexpr int cnt1(int s) {
  int n = 21;
  for (int j = 0; j <= s; ++j) n += n1(j);
  return n;
}
constexpr int pos_k(int i) { return cnt1(i) + n_x(i + 1) + 2; }
constexpr int wait1(int s) {
  int need = ((s & 1) && s >= 3 && s < 15) ? cnt1(s - 3) + 3 : s == 1 ? 20 : 0;
  if (s == 13) need = cmax(need, pos_k(7));
  if (s == 14) need = cmax(need, pos_k(11));
  return need ? cnt1(s - 1) - need : -1;
}

template <int N> DI void wait_vm() { asm volatile("s_waitcnt vmcnt(%0)" ::"n"(N) : "memory"); }
DI void raw_barrier() {
  asm volatile("s_waitcnt lgkmcnt(0)" ::: "memory");
  __builtin_amdgcn_s_barrier();
  asm volatile("" ::: "memory");
}
DI void dma16(unsigned voff, const void* sbase, unsigned lds_dst) {
  unsigned keep;
  asm volatile("s_mov_b32 %0, m0\n\ts_mov_b32 m0, %3\n\ts_nop 0\n\tglobal_load_lds_dwordx4 %1, %2\n\ts_mov_b32 m0, %0"
               : "=&s"(keep) : "v"(voff), "s"(sbase), "s"(lds_dst) : "memory");
}
DI void dma16_nt(unsigned voff, const void* sbase, unsigned lds_dst) {
  unsigned keep;
  asm volatile("s_mov_b32 %0, m0\n\ts_mov_b32 m0, %3\n\ts_nop 0\n\tglobal_load_lds_dwordx4 %1, %2 nt\n\ts_mov_b32 m0, %0"
               : "=&s"(keep) : "v"(voff), "s"(sbase), "s"(lds_dst) : "memory");
}
template <int LO, int HI> DI void touch(const char* a) {
  asm volatile("s_mov_b32 exec_lo, %1\n\ts_mov_b32 exec_hi, %2\n\tglobal_load_dword v255, %0, off\n\ts_mov_b64 exec, -1" ::"v"(a), "n"(LO), "n"(HI) : "v255", "memory");
}
DI void dma16_48(unsigned voff, const void* sbase, unsigned lds_dst) {
  unsigned keep;
  asm volatile("s_mov_b32 %0, m0\n\ts_mov_b32 m0, %3\n\ts_mov_b32 exec_hi, 0xffff\n\tglobal_load_lds_dwordx4 %1, %2\n\ts_mov_b32 exec_hi, -1\n\ts_mov_b32 m0, %0"
               : "=&s"(keep) : "v"(voff), "s"(sbase), "s"(lds_dst) : "memory");
}
DI void gload_nt(u32x4& d, unsigned voff, const void* sbase) {
  asm volatile("global_load_dwordx4 %0, %1, %2 nt" : "=v"(d) : "v"(voff), "s"(sbase) : "memory");
}
DI void mfma_acc(f32x4& c, bf16x8 a, bf16x8 b) {
  asm volatile("v_mfma_f32_16x16x32_bf16 %0, %1, %2, %0" : "+v"(c) : "v"(a), "v"(b));
}
DI void mfma_init(f32x4& c, bf16x8 a, bf16x8 b) {
  asm volatile("v_mfma_f32_16x16x32_bf16 %0, %1, %2, 0" : "=&v"(c) : "v"(a), "v"(b));
}
DI int lane_id() {
  int l;
  asm volatile("v_mbcnt_lo_u32_b32 %0, -1, 0\n\tv_mbcnt_hi_u32_b32 %0, -1, %0" : "=v"(l));
  return l;
}
template <int V> struct IC { static constexpr int value = V; };
typedef const __attribute__((address_space(3))) bf16x8* lds_frag_ptr;
typedef __attribute__((address_space(3))) u32x4* lds_u32x4_ptr;
DI bf16x8 lds_frag(unsigned a) { return *(lds_frag_ptr)(uintptr_t)a; }

typedef const volatile __attribute__((address_space(3))) s16x4* lds_half_ptr;
DI s16x4 lds_half(unsigned a) { return *(lds_half_ptr)(uintptr_t)a; }
template <int N, class F> DI void unroll(F&& f) {
  if constexpr (N > 0) {
    unroll<N - 1>(f);
    f(IC<N - 1>{});
  }
}
constexpr int ops0(int n) {
  return (n + 3 <= 15 ? 8 : 0) + (n >= 2 && !(n & 1) ? 2 : 0) + (n == 3) + (n == 6 || n == 8 || n == 10) + (n == 12);
}
constexpr int after0(int n) { return n == 12; }

__global__ __launch_bounds__(512, 2) void fused_kernel(
    const short* __restrict__ xA, const short* __restrict__ kA, const short* __restrict__ vA,
    const short* __restrict__ wqA, const short* __restrict__ woA, const short* __restrict__ xB,
    const short* __restrict__ kB, const short* __restrict__ vB, const short* __restrict__ wqB,
    const short* __restrict__ woB, char* p_ws, unsigned short* __restrict__ out) {
  extern __shared__ __attribute__((aligned(16))) char smem[];
  const int wave = __builtin_amdgcn_readfirstlane(threadIdx.x >> 6);
  const int bid = blockIdx.x;
  const int xcd = bid & 7, idx = bid >> 3;
  const int blk = xcd >> 2;
  const int hg = (xcd & 3) * 2 + (idx & 1);
  const int b = idx >> 1;
  const int h0 = hg * 4;
  const int u = wave >> 1, qh = wave & 1;

  const char* __restrict__ x = (const char*)(blk ? xB : xA) + (size_t)b * SQ * DM * 2;
  const char* __restrict__ kc = (const char*)(blk ? kB : kA) + ((size_t)b * HQ + h0) * (SQ * DH * 2);
  const char* __restrict__ vc = (const char*)(blk ? vB : vA) + ((size_t)b * HQ + h0) * (SQ * DH * 2);
  const char* __restrict__ wq = (const char*)(blk ? wqB : wqA) + h0 * DH * 2;
  const char* __restrict__ wo = (const char*)(blk ? woB : woA) + (size_t)h0 * DH * DM * 2;

  const unsigned lds0 = (unsigned)(uintptr_t)smem;
  STAMP(0);

  const int w4 = wave & 3;
  const int grp = blk * NB + b;
  const char* lines2 = p_ws + WS_LINES + (size_t)(grp * 32 + w4) * 256;
  const char* src2 = p_ws + (size_t)(((grp * 8 + hg) * 8 * 4 + w4) * 3072);
  auto pc_of = [&](int p) { return (hg + 1 + p) & 7; };
  auto to_mask = [&](int n) { return ((((1u << n) - 1u) * 0x101u) >> ((7 - hg) & 7)) & 0xffu; };
  auto from_mask = [&](int n) { return ((((1u << n) - 1u) * 0x101u) >> ((n - hg) & 7)) & 0xffu; };
  auto front = [&](auto rc) __attribute__((always_inline)) {
    constexpr int ROLE = decltype(rc)::value;
    const int lane = lane_id();
    const int c = lane & 15, g = lane >> 4, qp = (lane & 15) >> 2, p = lane & 3;
    unsigned vw[2], vx[2], vk[2], vv[2];
#pragma unroll
    for (int e = 0; e < 2; ++e) {
      const int f = g | (e << 2);
      const int ch = (((c >> 1) ^ f) << 1) | (c & 1);
      vw[e] = (unsigned)(g * (HD * 2) + w4 * (DH * 2) + ch * 16);
      const int r = 24 * w4 + 8 * e + (lane >> 3);
      vx[e] = (unsigned)((24 * w4 + (lane >> 3)) * (DM * 2) + (((lane & 7) ^ ((r >> 1) & 7)) << 4));
      const int row = 8 * w4 + 4 * e + g;
      vk[e] = (unsigned)(row * 256 + ((c ^ (row & 15)) << 4));
    }

    auto issue_w = [&](int st, int e) {
      dma16(vw[(e >> 1) & 1], wq + (size_t)(st * 32 + 4 * e) * (HD * 2), lds0 + (st % 3) * 32768 + (w4 * 8 + e) * 1024);
    };
    auto issue_x = [&](int j, int e) {
      dma16(vx[e & 1], x + j * 128 + e * 8 * (DM * 2), lds0 + XBASE + (j & 1) * 12288 + (24 * w4 + 8 * e) * 128);
    };
    auto piece_src = [&](int i) { return (i >= 12 ? vc : kc) + piece_u(i) * (SQ * DH * 2) + piece_pc(i) * 8192; };
    auto piece_dst = [&](int i, int e) { return lds0 + piece_blk(i) * 8192 + (w4 * 2 + e) * 1024; };
    auto dma_piece = [&](int i, int e) { dma16_nt(i >= 12 ? vv[e] : vk[e], piece_src(i), piece_dst(i, e)); };

    const int fl = qp | ((g & 1) << 2);
    unsigned trw[8], trw2[8];
#pragma unroll
    for (int dt = 0; dt < 8; ++dt) {
      trw[dt] = lds0 + u * 8192 + (8 * g + qp) * 256 + 8 * p + ((dt ^ fl) << 5);
      trw2[dt] = trw[dt] + 65536;
      asm volatile("" : "+v"(trw2[dt]));
    }
    unsigned xrd[2];
#pragma unroll
    for (int hf = 0; hf < 2; ++hf)
      xrd[hf] = lds0 + XBASE + (qh * 48 + c) * 128 + (((4 * hf + g) ^ ((c >> 1) & 7)) << 4);
    auto read_a = [&](int st, int dt) {
      const unsigned a0 = st % 3 == 2 ? trw2[dt] : trw[dt] + (st % 3) * 32768;
      return cat8(tr_read(a0), tr_read(a0 + 4 * 256));
    };
    auto read_x = [&](int st, int t) { return lds_frag(xrd[st & 1] + ((st >> 1) & 1) * 12288 + t * 16 * 128); };

    f32x4 qacc[8][3];
    bf16x8 af[8], xf[3];
    u32x4 kst[16], vst[4];
    unsigned seen = 0xffffffffu;

    const int tl = lane * 4 + w4;
    const char* ta = wq + (size_t)((tl >> 4) * 32 + 2 * b + ((tl >> 3) & 1)) * (HD * 2) + (tl & 7) * 128;
    if constexpr (ROLE == 0) {
#pragma unroll
      for (int e = 0; e < 3; ++e) issue_x(0, e);
#pragma unroll
      for (int e = 0; e < 8; ++e) issue_w(0, e);
      wait_vm<0>();
    } else {
      touch<0xffff0, 0>(ta);
      wait_vm<0>();
#pragma unroll
      for (int e = 8; e < 24; ++e) issue_w(e >> 3, e & 7);
#pragma unroll
      for (int e = 0; e < 3; ++e) issue_x(1, e);
      touch<0xf00000, 0>(ta);
    }
    raw_barrier();
    STAMP(1);
    STAMPW(2);
#pragma unroll
    for (int t = 0; t < 3; ++t) xf[t] = read_x(0, t);
#pragma unroll
    for (int dt = 0; dt < 8; ++dt) af[dt] = read_a(0, dt);

    auto mem_op = [&](auto sc, auto kc_) {
      constexpr int s = decltype(sc)::value, k = decltype(kc_)::value;
      if constexpr (ROLE == 0) {
        if constexpr (k < n_w(s)) issue_w(s + 3, k);
        else if constexpr (s == 13 && k < 4) gload_nt(vst[k], vv[k & 1], piece_src(12 + (k >> 1)));
        else if constexpr (s == 14 && k < 4) dma_piece(16 + (k >> 1), k & 1);
        else if constexpr (s == 15 && k < 4) dma_piece(20 + (k >> 1), k & 1);
      } else {
        constexpr int k2 = k - n_x(s);
        if constexpr (k2 < 0) {
          issue_x((s + 3) / 2, k);
        } else if constexpr (s >= 1 && s <= 12 && k2 < 2) {
          if constexpr (s - 1 < 4) dma_piece(s - 1, k2);
          else gload_nt(kst[(s - 5) * 2 + k2], vk[k2], piece_src(s - 1));
        } else if constexpr (s <= 8 && k2 == (s == 0 ? 0 : 2)) {
          constexpr unsigned long long m = (s == 8 ? 255ull : 15ull) << (4 * (s + 6));
          touch<(int)(unsigned)m, (int)(unsigned)(m >> 32)>(ta);
        } else if constexpr (s == 2 && k2 == 3) {
          asm volatile("s_mov_b64 exec, 0xffff\n\tglobal_load_dword %0, %1, %2 sc1\n\ts_mov_b64 exec, -1"
                       : "+v"(seen) : "v"(lane * 16), "s"(lines2 + hg * 1024) : "memory");
        } else if constexpr ((s == 2 && k2 >= 4 && k2 < 6) || (s == 4 && k2 >= 3 && k2 < 7) || (s == 8 && k2 >= 3 && k2 < 6)) {
          constexpr int j = s == 2 ? k2 - 4 : s == 4 ? k2 - 1 : k2 + 3;
          asm volatile("global_store_dwordx4 %0, %1, %2 offset:%3 sc1\n\ts_nop 1" ::"v"(lane * 16), "v"((u32x4){EMPTY, EMPTY, EMPTY, EMPTY}),
                       "s"(src2 + (5 + j / 3) * 12288), "n"((j % 3) * 1024) : "memory");
        } else if constexpr (s == 6 && k2 == 3) {
          asm volatile("" : "+v"(seen));
          unsigned j = 0;
          while (__builtin_amdgcn_ballot_w64((seen >> 1) == j) != 0) ++j;
          asm volatile("s_mov_b64 exec, 3\n\tglobal_store_dword %0, %1, %2 sc1\n\ts_mov_b64 exec, -1"
                       ::"v"(lane * 128), "v"(2 * j), "s"(lines2 + hg * 1040) : "memory");
        } else if constexpr (s == 13 && k < 8) {
          *(lds_u32x4_ptr)(uintptr_t)(piece_dst(4 + (k >> 1), k & 1) + lane * 16) = kst[k];
        } else if constexpr (s == 13 && k < 12) {
          gload_nt(vst[k - 8], vv[k & 1], piece_src(14 + ((k - 8) >> 1)));
        } else if constexpr (s == 14 && k < 8) {
          *(lds_u32x4_ptr)(uintptr_t)(piece_dst(8 + (k >> 1), k & 1) + lane * 16) = kst[8 + k];
        } else if constexpr (s == 15 && k < 8) {
          dma_piece(18 + (k >> 1) + (k >= 4 ? 2 : 0), k & 1);
        }
      }
    };
    auto group = [&](auto sc, auto dc, bf16x8* an, bf16x8* xn) {
      constexpr int s = decltype(sc)::value, dt = decltype(dc)::value;
      constexpr int n = ROLE == 0 ? n0(s) : (s == 13 ? 12 : s == 14 ? 8 : n1(s));
      constexpr int per = n <= 8 ? 1 : 2;
#pragma unroll
      for (int t = 0; t < 3; ++t) {
        if constexpr (s == 0) mfma_init(qacc[dt][t], af[dt], xf[t]);
        else mfma_acc(qacc[dt][t], af[dt], xf[t]);
      }
      if constexpr (s > 0) {
        if constexpr (dt < 7) an[dt] = read_a(s + 1, dt);
        if constexpr (dt >= 1 && dt <= 3) xn[dt - 1] = read_x(s + 1, dt - 1);
        if constexpr (dt == 6) an[7] = read_a(s + 1, 7);
      }
      mem_op(sc, IC<dt * per>{});
      if constexpr (per > 1) mem_op(sc, IC<dt * per + 1>{});
      __builtin_amdgcn_sched_barrier(0);
    };
    auto step = [&](auto ic) {
      constexpr int s = decltype(ic)::value;
      if constexpr (ROLE == 0 && s > 1) wait_vm<wait0(s)>();
      if constexpr (ROLE == 1 && wait1(s) >= 0) wait_vm<wait1(s)>();
      raw_barrier();
      STAMP(3 + s);
      bf16x8 xn[3], an[8];
      group(ic, IC<0>{}, an, xn); group(ic, IC<1>{}, an, xn); group(ic, IC<2>{}, an, xn); group(ic, IC<3>{}, an, xn);
      group(ic, IC<4>{}, an, xn); group(ic, IC<5>{}, an, xn); group(ic, IC<6>{}, an, xn); group(ic, IC<7>{}, an, xn);
      if constexpr (s == 0) {
        if constexpr (ROLE == 1) wait_vm<cnt1(0) - 9>();
        raw_barrier();
#pragma unroll
        for (int t = 0; t < 3; ++t) xn[t] = read_x(1, t);
#pragma unroll
        for (int dt = 0; dt < 8; ++dt) an[dt] = read_a(1, dt);
      }
#pragma unroll
      for (int t = 0; t < 3; ++t) xf[t] = xn[t];
#pragma unroll
      for (int dt = 0; dt < 8; ++dt) af[dt] = an[dt];
    };
    step(IC<0>{}); step(IC<1>{}); step(IC<2>{}); step(IC<3>{}); step(IC<4>{}); step(IC<5>{}); step(IC<6>{}); step(IC<7>{});
    step(IC<8>{}); step(IC<9>{}); step(IC<10>{}); step(IC<11>{}); step(IC<12>{});
    {
      const int l = lane_id();
#pragma unroll
      for (int e = 0; e < 2; ++e) {
        const int row = 8 * w4 + 4 * e + (l >> 4);
        vv[e] = (unsigned)(row * 256 + ((((((l & 15) >> 1) ^ (row & 7)) << 1) | (l & 1)) << 4));
      }
    }
    step(IC<13>{}); step(IC<14>{});

    raw_barrier();
    STAMP(18);
    const int lane1 = lane_id();
    const int c1 = lane1 & 15, g1 = lane1 >> 4, qp1 = (lane1 & 15) >> 2, p1 = lane1 & 3;
    const unsigned l16 = (unsigned)lane1 * 16;
    u32x4 qf[4][3];
    bf16x8 kf[6][4];
    const unsigned kl = (unsigned)(c1 * 256 + (((g1 >> 1) ^ c1) << 4) + (g1 & 1) * 8);
    const unsigned kbase = lds0 + u * 8192;
    auto read_k = [&](auto jc) {
      constexpr int j = decltype(jc)::value;
      if constexpr (j < 24) {
        constexpr int s = j / 6, kt = j % 6;
        constexpr unsigned rb = kt < 2 ? 131072u : 32768u, off = (kt >= 4 ? 32768u : 0u) + (kt & 1) * 4096u;
        kf[kt][s] = cat8(lds_half((kl ^ (s << 6)) + (kbase + rb) + off), lds_half((kl ^ ((s << 6) | 32)) + (kbase + rb) + off));
      }
    };
    auto q_cvt = [&](int dt) {
#pragma unroll
      for (int t = 0; t < 3; ++t) {
        qf[dt >> 1][t][(dt & 1) * 2] = pack2(qacc[dt][t][0], qacc[dt][t][1]);
        qf[dt >> 1][t][(dt & 1) * 2 + 1] = pack2(qacc[dt][t][2], qacc[dt][t][3]);
      }
    };
    unroll<8>([&](auto dc) {
      constexpr int dt = decltype(dc)::value;
#pragma unroll
      for (int t = 0; t < 3; ++t) qacc[dt][t] = MFMA16(af[dt], xf[t], qacc[dt][t]);
      if constexpr (dt >= 2) q_cvt(dt - 2);
      if constexpr (dt == 7) q_cvt(6);
      read_k(dc);
      mem_op(IC<15>{}, dc);
      __builtin_amdgcn_sched_barrier(0);
    });

    const f32x4 zero4 = {0.f, 0.f, 0.f, 0.f};
    const float cexp = 0.08838834764831845f * 1.4426950408889634f;
    f32x4 sacc[6][3], oacc[8][3], lsum[3];
    float mx[3], ex[3][24], rinv[3];
    u32x4 pf[3][3];
    bf16x8 vf[3][8], of[2][2][4][3];
    auto s_mm = [&](auto tc, auto jc) {
      constexpr int t = decltype(tc)::value, j = decltype(jc)::value, s = j / 6, kt = j % 6;
      sacc[kt][t] = MFMA16(kf[kt][s], __builtin_bit_cast(bf16x8, qf[s][t]), s == 0 ? zero4 : sacc[kt][t]);
    };
    auto m_op = [&](auto tc, auto qc) {
      constexpr int t = decltype(tc)::value, q = decltype(qc)::value;
      auto sv = [&](int j) { return sacc[j >> 2][t][j & 3]; };
      if constexpr (q < 4) {
#pragma unroll
        for (int i = 3 * q; i < 3 * q + 3; ++i) {
          if (i == 0) mx[t] = fmaxf(fmaxf(sv(0), sv(1)), sv(2));
          else if (i == 11) mx[t] = fmaxf(mx[t], sv(23));
          else mx[t] = fmaxf(fmaxf(mx[t], sv(2 * i + 1)), sv(2 * i + 2));
        }
      } else if constexpr (q == 4) {
        auto s = __builtin_amdgcn_permlane16_swap(__float_as_uint(mx[t]), __float_as_uint(mx[t]), false, false);
        mx[t] = fmaxf(__uint_as_float(s[0]), __uint_as_float(s[1]));
      } else if constexpr (q == 5) {
        auto s = __builtin_amdgcn_permlane32_swap(__float_as_uint(mx[t]), __float_as_uint(mx[t]), false, false);
        mx[t] = fmaxf(__uint_as_float(s[0]), __uint_as_float(s[1])) * -cexp;
      }
#pragma unroll
      for (int j2 = 0; j2 < 12; ++j2) {
        const int st = 6 + (j2 * 15) / 11;
        if (q == st + 2) pf[j2 >> 2][t][j2 & 3] = pack2(ex[t][2 * j2], ex[t][2 * j2 + 1]);
        if (q == st + 1) {
          ex[t][2 * j2] = __builtin_amdgcn_exp2f(ex[t][2 * j2]);
          ex[t][2 * j2 + 1] = __builtin_amdgcn_exp2f(ex[t][2 * j2 + 1]);
        }
        if (q == st) {
          ex[t][2 * j2] = fmaf(sv(2 * j2), cexp, mx[t]);
          ex[t][2 * j2 + 1] = fmaf(sv(2 * j2 + 1), cexp, mx[t]);
        }
      }
    };
    const int fl1 = qp1 | ((g1 & 1) << 2);
    const unsigned vl = (unsigned)((4 * g1 + qp1) * 256 + 8 * p1 + (fl1 << 5));
    auto read_v = [&](auto ic) {
      constexpr int i = decltype(ic)::value, s = i < 8 ? 0 : i < 16 ? 2 : 1, dt = i & 7;
      const unsigned a = (vl ^ (dt << 5)) + (kbase + (s == 1 ? 0u : 65536u)) + (s == 2 ? 32768u : 0u);
      vf[s][dt] = cat8(tr_read(a), tr_read(a + 16 * 256));
    };
    u32x4 ones = {0x3f803f80u, 0x3f803f80u, 0x3f803f80u, 0x3f803f80u};
    auto p_mm = [&](auto tc, auto jc) {
      constexpr int t = decltype(tc)::value, j = decltype(jc)::value, s = j < 9 ? 0 : j < 18 ? 2 : 1, dt = j % 9;
      const bf16x8 pb = __builtin_bit_cast(bf16x8, pf[s][t]);
      if constexpr (dt == 8) lsum[t] = MFMA16(__builtin_bit_cast(bf16x8, ones), pb, j < 9 ? zero4 : lsum[t]);
      else oacc[dt][t] = MFMA16(vf[s][dt], pb, j < 9 ? zero4 : oacc[dt][t]);
    };
    const unsigned ow = lds0 + wave * 4096 + l16;
    auto o_op = [&](auto tc, auto qc) {
      constexpr int t = decltype(tc)::value, q = decltype(qc)::value;
      if constexpr (q == 0) {
        rinv[t] = __builtin_amdgcn_rcpf(lsum[t][0]);
      } else if constexpr (q <= 12) {
        constexpr int s = (q - 1) / 3, k = (q - 1) % 3;
        if constexpr (k < 2) {
#pragma unroll
          for (int r = 0; r < 4; ++r) oacc[2 * s + k][t][r] *= rinv[t];
        } else {
          u32x4 r;
          r[0] = pack2(oacc[2 * s][t][0], oacc[2 * s][t][1]);
          r[1] = pack2(oacc[2 * s][t][2], oacc[2 * s][t][3]);
          r[2] = pack2(oacc[2 * s + 1][t][0], oacc[2 * s + 1][t][1]);
          r[3] = pack2(oacc[2 * s + 1][t][2], oacc[2 * s + 1][t][3]);
          *(lds_u32x4_ptr)(uintptr_t)(ow + o_reg(t) + s * 1024) = r;
          of[ROLE][0][s][t] = __builtin_bit_cast(bf16x8, r);
        }
      }
    };
    const unsigned ord0 = lds0 + qh * 4096 + (u & 1) * 8192 + l16;
    auto read_o = [&](auto tc, auto ic) {
      constexpr int t = decltype(tc)::value, i = decltype(ic)::value;
      constexpr int half = i < 4 ? ROLE : 1 - ROLE, hh = i < 4 ? 1 : (i - 4) >> 2, s = i & 3;
      of[half][hh][s][t] = lds_frag((ord0 ^ (hh * 8192)) + o_reg(t) + (half * 16384 + s * 1024));
    };
    unsigned vo;
    {
      const int srow = lane1 >> 3, pos = lane1 & 7;
      vo = (unsigned)((w4 * 64 + srow) * (DM * 2) + (((((pos >> 1) ^ ((srow >> 1) & 3)) << 1) | (pos & 1)) << 4));
    }
    auto issue_wo = [&](int st, int e) {
      dma16(vo, wo + (size_t)((st & 1) * 256 + e * 8) * (DM * 2) + pc_of(st >> 1) * 128, lds0 + st_reg(st) + (w4 * 64 + e * 8) * 128);
    };
    const char* lines = lines2 + ROLE * 128;
    const char* src = src2 + ROLE * 1536;
    const unsigned others = 0xffu & ~(1u << hg);
    unsigned tag = 0, mk = 0, pl = 0;
    auto signal = [&](unsigned v, unsigned m) {
      unsigned a;
      asm volatile("v_lshlrev_b32 %0, 6, %1\n\ts_mov_b32 exec_lo, %4\n\ts_mov_b32 exec_hi, 0\n\tglobal_store_dword %0, %2, %3 sc1\n\ts_mov_b64 exec, -1"
                   : "=&v"(a) : "v"(l16), "v"(v), "s"(lines + hg * 16), "s"(m) : "memory");
    };
    auto poll = [&](unsigned& f) {
      asm volatile("s_mov_b64 exec, 0xff\n\tglobal_load_dword %0, %1, %2 sc1\n\ts_mov_b64 exec, -1"
                   : "+v"(f) : "v"(l16), "s"(lines + hg * 1024) : "memory");
    };
    auto marks = [&](unsigned& m) {
      unsigned a;
      asm volatile("v_mul_u32_u24 %1, %4, %2\n\ts_mov_b64 exec, 0xff\n\tglobal_load_dword %0, %1, %3 sc1\n\ts_mov_b64 exec, -1"
                   : "+v"(m), "=&v"(a) : "v"(l16), "s"(lines), "n"(65) : "memory");
    };
    auto heard = [&](unsigned& f) {
      asm volatile("" : "+v"(f));
      const unsigned got = (unsigned)__builtin_amdgcn_ballot_w64((f ^ __builtin_amdgcn_readlane(f, hg)) < 2u);
      return (got & 0xffu) == 0xffu;
    };
    auto ready = [&](unsigned& f, unsigned need) {
      asm volatile("" : "+v"(f));
      const unsigned got = (unsigned)__builtin_amdgcn_ballot_w64(f == __builtin_amdgcn_readlane(f, hg) + 1u);
      return (got & need) == need;
    };
    auto retag = [&](unsigned& m) {
      asm volatile("" : "+v"(m));
      const unsigned bad = (unsigned)__builtin_amdgcn_ballot_w64(m != tag) & others;
      tag = m;
      if (bad) signal(tag, bad);
    };

    unroll<24>([&](auto jc) {
      constexpr int j = decltype(jc)::value;
      s_mm(IC<0>{}, jc);
      if constexpr (j == 1) q_cvt(7);
      read_k(IC<j + 8>{});
      __builtin_amdgcn_sched_barrier(0);
    });
    wait_vm<8>();
    raw_barrier();
    STAMP(19);
    unroll<24>([&](auto jc) {
      constexpr int j = decltype(jc)::value;
      s_mm(IC<1>{}, jc);
      if constexpr (j < 4) *(lds_u32x4_ptr)(uintptr_t)(piece_dst(12 + 2 * ROLE + (j >> 1), j & 1) + l16) = vst[j];
      if constexpr (ROLE == 1 && j == 4) marks(tag);
      if constexpr (j == 5) {
        const int tw = (lane1 & 31) * 8 + wave;
        touch<-1, 0>(wo + ((tw >> 4) * 32 + 2 * b + ((tw >> 3) & 1)) * (DM * 2) + (tw & 7) * 128);
      }
      m_op(IC<0>{}, jc);
      __builtin_amdgcn_sched_barrier(0);
    });
    raw_barrier();
    STAMP(20);
    unroll<24>([&](auto jc) {
      constexpr int j = decltype(jc)::value;
      s_mm(IC<2>{}, jc);
      m_op(IC<1>{}, jc);
      if constexpr (j >= 8 && j < 24 && !(j & 1)) read_v(IC<(j - 8) / 2>{});
      __builtin_amdgcn_sched_barrier(0);
    });
    wait_vm<1>();
    raw_barrier();
    STAMP(21);
    unroll<27>([&](auto jc) {
      constexpr int j = decltype(jc)::value;
      p_mm(IC<0>{}, jc);
      if constexpr (j < 16) read_v(IC<j + 8>{});
      if constexpr (ROLE == 1 && j == 0) { asm volatile("" : "+v"(tag)); signal(tag, others); }
      if constexpr (j >= 2 && j < 26) m_op(IC<2>{}, IC<j - 2>{});
      if constexpr (j % 3 == 1 && j < 24) issue_wo(ROLE, j / 3);
      __builtin_amdgcn_sched_barrier(0);
    });
    raw_barrier();
    STAMP(22);
    unroll<27>([&](auto jc) {
      constexpr int j = decltype(jc)::value;
      p_mm(IC<1>{}, jc);
      if constexpr (j & 1) o_op(IC<0>{}, IC<j / 2>{});
      __builtin_amdgcn_sched_barrier(0);
    });
    raw_barrier();
    STAMP(23);
    unroll<27>([&](auto jc) {
      constexpr int j = decltype(jc)::value;
      p_mm(IC<2>{}, jc);
      if constexpr (j & 1) o_op(IC<1>{}, IC<j / 2>{});
      if constexpr (j >= 2 && j < 26 && !(j & 1)) read_o(IC<0>{}, IC<(j - 2) / 2>{});
      __builtin_amdgcn_sched_barrier(0);
    });
    wait_vm<0>();
    raw_barrier();
    STAMP(24);

    auto sidx = [](int n) { return n < 2 ? n ^ ROLE : n; };
    unsigned wa[2];
    wa[0] = lds0 + st_reg(sidx(0)) + (u & 1) * 16384 + (4 * g1 + qp1) * 128 + 8 * p1 + ((u ^ ((2 * g1 + (qp1 >> 1)) & 3)) << 5);
    wa[1] = wa[0] ^ 16384u;
    asm volatile("" : "+v"(wa[0]), "+v"(wa[1]));
    bf16x8 wf[4];
    auto read_wo = [&](auto fc) {
      constexpr int f = decltype(fc)::value, n = f >> 3, hh = (f >> 2) & 1, ks = f & 3;
      if constexpr (n < 16) {
        if constexpr (n > 0 && ks == 0)
          asm volatile("v_add_u32 %0, %1, %0" : "+v"(wa[hh]) : "n"(st_reg(n < 2 ? n ^ ROLE : n) - st_reg(n - 1 < 2 ? (n - 1) ^ ROLE : n - 1)));
        wf[f & 3] = cat8(tr_read(wa[hh] + ks * 4096), tr_read(wa[hh] + ks * 4096 + 16 * 128));
      }
    };
    unroll<13>([&](auto jc) {
      constexpr int j = decltype(jc)::value;
      if constexpr (j < 12) read_o(IC<1>{}, jc);
      o_op(IC<2>{}, jc);
      if constexpr (ROLE == 0 && j < 8) issue_wo(2, j);
      if constexpr (j >= 10) read_wo(IC<j - 10>{});
      __builtin_amdgcn_sched_barrier(0);
    });
    raw_barrier();
    STAMP(25);

    f32x4 acc[2][3];
    u32x4 in4[7];
    u32x2 in2[7];
    unsigned early = 0;
    const unsigned l8 = l16 >> 1;
    auto load_in = [&](int k) {
      asm volatile("global_load_dwordx4 %0, %2, %4 sc1\n\tglobal_load_dwordx2 %1, %3, %4 offset:1024 sc1"
                   : "=&v"(in4[k - 1]), "=&v"(in2[k - 1]) : "v"(l16), "v"(l8), "s"(src + k * 12288) : "memory");
    };
    auto oint = [&](auto nc) {
      constexpr int n = decltype(nc)::value;
      constexpr int half = (n < 2 ? n ^ ROLE : n) & 1, pb = (n >> 1) & 1;
      unroll<24>([&](auto ic) {
        constexpr int i = decltype(ic)::value, f = i / 3, t = i % 3;
        if constexpr (!(n & 1) && f == 0) mfma_init(acc[pb][t], wf[f & 3], of[half][f >> 2][f & 3][t]);
        else mfma_acc(acc[pb][t], wf[f & 3], of[half][f >> 2][f & 3][t]);
        if constexpr (t == 0) read_wo(IC<n * 8 + f + 3>{});
        if constexpr (n == 0 && i < 12) read_o(IC<2>{}, ic);
        if constexpr (ROLE == 0 && t == 0 && n + 3 <= 15) issue_wo(n + 3, f);
        if constexpr (ROLE == 1 && n == 0 && i == 1) poll(pl);
        if constexpr (n == 3 && i == 1) marks(mk);
        if constexpr ((n == 6 || n == 8 || n == 10) && i == 1) {
          if constexpr (ROLE == 1) wait_vm<0>();
          if constexpr (n == 6) {
            if constexpr (ROLE == 1) retag(mk);
            asm volatile("" : "+v"(mk));
            tag = mk + 1u;
          }
          signal(tag, n == 6 ? to_mask(2) : 1u << pc_of(n / 2 - 2));
        }
        if constexpr (n >= 2 && !(n & 1) && i == 4) {
          constexpr int p = n / 2 - 1;
          const char* dst = p_ws + (size_t)((((grp * 8 + pc_of(p)) * 8 + p + 1) * 4 + w4) * 3072 + ROLE * 1536);
          u32x4 r;
          u32x2 r2;
          r[0] = pack2(acc[1 - pb][0][0], acc[1 - pb][0][1]); r[1] = pack2(acc[1 - pb][0][2], acc[1 - pb][0][3]);
          r[2] = pack2(acc[1 - pb][1][0], acc[1 - pb][1][1]); r[3] = pack2(acc[1 - pb][1][2], acc[1 - pb][1][3]);
          r2[0] = pack2(acc[1 - pb][2][0], acc[1 - pb][2][1]); r2[1] = pack2(acc[1 - pb][2][2], acc[1 - pb][2][3]);
          asm volatile("global_store_dwordx4 %0, %2, %4 sc1\n\tglobal_store_dwordx2 %1, %3, %4 offset:1024 sc1\n\ts_nop 1"
                       ::"v"(l16), "v"(l8), "v"(r), "v"(r2), "s"(dst) : "memory");
        }
        if constexpr (n == 12 && i == 23) poll(pl);
        if constexpr (n == 15 && i == 0) {
          wait_vm<2>();
          early = __builtin_amdgcn_readfirstlane(ready(pl, from_mask(4)) ? 1u : 0u);
        }
        if constexpr (n == 15 && i >= 1 && i <= 6) load_in(i);
        if constexpr (n == 15 && i == 16) load_in(7);
        __builtin_amdgcn_sched_barrier(0);
      });
      if constexpr (ROLE == 1 && n == 2) {
        wait_vm<2>();
        if (!heard(pl)) {
#pragma unroll 1
          for (int it = 0; it < SPIN_CAP; ++it) {
            unsigned m = tag;
            poll(pl);
            marks(m);
            wait_vm<0>();
            retag(m);
            if (heard(pl)) break;
            __builtin_amdgcn_s_sleep(1);
          }
        }
      }
      if constexpr (n < 14) {
        if constexpr (ROLE == 0) wait_vm<ops0(n) + (n > 0 ? after0(n - 1) : 0)>();
        raw_barrier();
        if constexpr (n & 1) { STAMP(26 + n / 2); }
      }
    };
    unroll<16>(oint);
    asm volatile("s_nop 7\n\ts_nop 3" ::: "memory");
    STAMPW(33);

    auto add_in = [&](int k) {
      asm volatile("" : "+v"(in4[k - 1]), "+v"(in2[k - 1]));
#pragma unroll
      for (int e = 0; e < 6; ++e) {
        const unsigned w = e < 4 ? in4[k - 1][e & 3] : in2[k - 1][e & 1];
        acc[1][e >> 1][2 * (e & 1)] += __uint_as_float(w << 16);
        acc[1][e >> 1][2 * (e & 1) + 1] += __uint_as_float(w & 0xffff0000u);
      }
      asm volatile("" : "+v"(acc[1][0]), "+v"(acc[1][1]), "+v"(acc[1][2]));
    };
    auto whole = [&]() {
      bool ok = true;
#pragma unroll
      for (int k = 4; k < 7; ++k) {
        asm volatile("" : "+v"(in4[k]), "+v"(in2[k]));
        ok = ok && in4[k][0] != EMPTY && in2[k][0] != EMPTY;
      }
      return __builtin_amdgcn_ballot_w64(!ok) == 0;
    };
    wait_vm<6>();
    if (!early) {
#pragma unroll 1
      for (int it = 0; it < SPIN_CAP; ++it) {
        poll(pl);
        wait_vm<0>();
        if (ready(pl, from_mask(4))) break;
        __builtin_amdgcn_s_sleep(1);
      }
      load_in(1); load_in(2); load_in(3); load_in(4);
      wait_vm<0>();
    }
    add_in(1); add_in(2); add_in(3); add_in(4);
    STAMPW(34);
    wait_vm<0>();
    if (!whole()) {
#pragma unroll 1
      for (int it = 0; it < SPIN_CAP; ++it) {
        load_in(5); load_in(6); load_in(7);
        wait_vm<0>();
        if (whole()) break;
      }
    }
    add_in(5); add_in(6); add_in(7);
    STAMPW(35);
    {
      const int lane2 = lane_id();
      unsigned short* o = out + (size_t)(b * SQ + qh * 48 + (lane2 & 15)) * 1024 + blk * 512 + hg * 64 + u * 16 + 4 * (lane2 >> 4);
#pragma unroll
      for (int t = 0; t < 3; ++t) {
        u32x2 r;
        r[0] = pack2(acc[1][t][0], acc[1][t][1]);
        r[1] = pack2(acc[1][t][2], acc[1][t][3]);
        *(u32x2*)(o + t * 16 * 1024) = r;
      }
    }
    STAMPW(36);
    STAMPEND(37);
  };
  if (wave >= 4) {
    front(IC<1>{});
    __builtin_amdgcn_endpgm();
  }
  front(IC<0>{});
}

}

extern "C" void kernel_launch(void* const* d_in, const int* in_sizes, int n_in, void* d_out, int out_size,
                              void* d_ws, size_t ws_size, hipStream_t stream) {
  const short* xA = (const short*)d_in[0];
  const short* kA = (const short*)d_in[1];
  const short* vA = (const short*)d_in[2];
  const short* wqA = (const short*)d_in[3];
  const short* woA = (const short*)d_in[4];
  const short* xB = (const short*)d_in[5];
  const short* kB = (const short*)d_in[6];
  const short* vB = (const short*)d_in[7];
  const short* wqB = (const short*)d_in[8];
  const short* woB = (const short*)d_in[9];
  (void)hipFuncSetAttribute((const void*)fused_kernel, hipFuncAttributeMaxDynamicSharedMemorySize, K1_LDS);
  hipLaunchKernelGGL(fused_kernel, dim3(256), dim3(512), K1_LDS, stream, xA, kA, vA, wqA, woA, xB, kB, vB, wqB,
                     woB, (char*)d_ws, (unsigned short*)d_out);
}
```

```cpp
#include <hip/hip_runtime.h>
#include <hip/hip_bf16.h>
#include <stdint.h>

typedef __attribute__((ext_vector_type(8))) short bf16x8;
typedef __attribute__((ext_vector_type(4))) short s16x4;
typedef __attribute__((ext_vector_type(4))) float f32x4;
typedef __attribute__((ext_vector_type(2))) float f32x2;
typedef __attribute__((ext_vector_type(4))) unsigned u32x4;
typedef __attribute__((ext_vector_type(2))) unsigned u32x2;
typedef __attribute__((ext_vector_type(2))) __bf16 bf16x2_t;
typedef __attribute__((address_space(3))) s16x4* lds_s16x4_ptr;

#define DI __device__ __forceinline__
#define MFMA16(a, b, c) __builtin_amdgcn_mfma_f32_16x16x32_bf16((a), (b), (c), 0, 0, 0)

#define XS(k)
#define STAMP(i)
#define STAMPW(i)
#define STAMPEND(i)
namespace {

constexpr int NB = 16, SQ = 96, DM = 512, HQ = 32, DH = 128, HD = HQ * DH;

DI unsigned pack2(float a, float b) {
  f32x2 v = {a, b};
  return __builtin_bit_cast(unsigned, __builtin_convertvector(v, bf16x2_t));
}
DI s16x4 tr_read(unsigned lds_addr) {
  return __builtin_amdgcn_ds_read_tr16_b64_v4i16((lds_s16x4_ptr)(uintptr_t)lds_addr);
}
DI bf16x8 cat8(s16x4 lo, s16x4 hi) { return __builtin_shufflevector(lo, hi, 0, 1, 2, 3, 4, 5, 6, 7); }
DI float xg_max(float x) {
  auto s = __builtin_amdgcn_permlane16_swap(__float_as_uint(x), __float_as_uint(x), false, false);
  x = fmaxf(__uint_as_float(s[0]), __uint_as_float(s[1]));
  auto t = __builtin_amdgcn_permlane32_swap(__float_as_uint(x), __float_as_uint(x), false, false);
  return fmaxf(__uint_as_float(t[0]), __uint_as_float(t[1]));
}
DI float xg_sum(float x) {
  auto s = __builtin_amdgcn_permlane16_swap(__float_as_uint(x), __float_as_uint(x), false, false);
  x = __uint_as_float(s[0]) + __uint_as_float(s[1]);
  auto t = __builtin_amdgcn_permlane32_swap(__float_as_uint(x), __float_as_uint(x), false, false);
  return __uint_as_float(t[0]) + __uint_as_float(t[1]);
}

constexpr int K1_LDS = 160 * 1024;
constexpr int XBASE = 96 * 1024;

constexpr int piece_u(int i) {
  constexpr int t[24] = {0, 1, 2, 3, 0, 1, 2, 3, 0, 1, 2, 3, 0, 1, 2, 3, 3, 0, 0, 1, 2, 3, 1, 2};
  return t[i];
}
constexpr int piece_pc(int i) {
  constexpr int t[24] = {0, 0, 0, 0, 1, 1, 1, 1, 2, 2, 2, 2, 0, 0, 0, 0, 2, 2, 1, 1, 1, 1, 2, 2};
  return t[i];
}
constexpr int piece_blk(int i) {
  const int pc = piece_pc(i);
  return (i < 12 ? (pc == 0 ? 16 : pc == 1 ? 4 : 8) : (pc == 0 ? 8 : pc == 1 ? 0 : 12)) + piece_u(i);
}
constexpr unsigned o_reg(int t) { return t == 0 ? 0u : t == 1 ? 65536u : 98304u; }
constexpr unsigned st_reg(int st) {
  constexpr unsigned r[5] = {32768u, 131072u, 0u, 65536u, 98304u};
  return r[st % 5];
}
constexpr unsigned EMPTY = 0x7f817f81u;
constexpr unsigned WS_LINES = 32u << 20;
constexpr int SPIN_CAP = 1 << 16;

enum { K_W, K_X, K_PD, K_PR, K_PW, K_T, K_SEEN, K_PUB };
struct Op { int step, slot, kind, a, b, r, due; };
struct Sched {
  Op op[2][200];
  int n[2];
  constexpr void add(int role, int step, int slot, int kind, int a, int b, int r, int due) {
    op[role][n[role]] = Op{step, slot, kind, a, b, r, due};
    ++n[role];
  }
};
#define KPS 1
#define KS0 1
#define R1PRE 0
#define R0SH 0
#define TPS 1
constexpr Sched make_sched() {
  Sched S{};
  for (int e = 0; e < 3; ++e) S.add(0, -1, 0, K_X, 0, e, 0, -1);
  for (int e = 0; e < 8; ++e) S.add(0, -1, 0, K_W, 0, e, 0, -1);
  S.add(1, -1, 0, K_T, 1, 4, 0, -1);
  for (int e = 8; e < 24; ++e) S.add(1, -1, 1, K_W, e >> 3, e & 7, 0, (e >> 3) - 1);
  for (int e = 0; e < 3; ++e) S.add(1, -1, 1, K_X, 1, e, 0, 1);
  S.add(1, -1, 1, K_T, 5, 1, 0, 99);
  int kp = 0, tw = 6;
  for (int s = 0; s < 16; ++s) {
    int k = 0;
    auto slot0 = [&](int i, int n) { return n <= 8 - R0SH ? i + R0SH : R0SH + i * (8 - R0SH) / n; };
    if (s + 3 <= 15) for (int e = 0; e < 8; ++e) S.add(0, s, slot0(e, 8), K_W, s + 3, e, 0, s + 2);
    if (s == 13) for (int e = 0; e < 4; ++e) S.add(0, s, e, K_PR, 12 + (e >> 1), e & 1, 16 + e, 99);
    if (s == 14) for (int e = 0; e < 4; ++e) S.add(0, s, e, K_PD, 16 + (e >> 1), e & 1, 0, 99);
    if (s == 15) for (int e = 0; e < 4; ++e) S.add(0, s, e, K_PD, 20 + (e >> 1), e & 1, 0, 99);
    const int first = S.n[1];
    if ((s & 1) && (s + 3) / 2 <= 7) for (int e = 0; e < 3; ++e) S.add(1, s, 0, K_X, (s + 3) / 2, e, 0, s + 2);
    if (s >= KS0)
      for (int i = 0; i < KPS && kp < 12; ++i, ++kp)
        for (int e = 0; e < 2; ++e) {
          if (kp < 4) S.add(1, s, 0, K_PD, kp, e, 0, 15);
          else S.add(1, s, 0, K_PR, kp, e, (kp - 4) * 2 + e, kp < 8 ? 13 : 14);
        }
    if (tw < 16) {
      const int c = tw + TPS + 1 >= 16 ? 16 - tw : TPS;
      S.add(1, s, 0, K_T, tw, c, 0, 11);
      tw += c;
    }
    if (s == 2) S.add(1, s, 0, K_SEEN, 0, 0, 0, 6);
    if (s == 6) S.add(1, s, 0, K_PUB, 0, 0, 0, 13);
    if (s == 13 || s == 14) for (int e = 0; e < 8; ++e) S.add(1, s, 0, K_PW, (s == 13 ? 4 : 8) + (e >> 1), e & 1, (s - 13) * 8 + e, 99);
    if (s == 13) for (int e = 0; e < 4; ++e) S.add(1, s, 0, K_PR, 14 + (e >> 1), e & 1, 16 + e, 99);
    if (s == 15) for (int e = 0; e < 8; ++e) S.add(1, s, 0, K_PD, 18 + (e >> 1) + (e >= 4 ? 2 : 0), e & 1, 0, 99);
    const int n = S.n[1] - first, pre = s < 13 ? (n < R1PRE ? n : R1PRE) : 0;
    for (int i = 0; i < n; ++i) S.op[1][first + i].slot = i < pre ? -1 : (n - pre <= 8 ? i - pre : (i - pre) * 8 / (n - pre));
    (void)k;
  }
  return S;
}
constexpr Sched SCH = make_sched();
constexpr int vm_cnt(int role, int step) {
  int c = 0;
  for (int i = 0; i < SCH.n[role]; ++i) c += SCH.op[role][i].step <= step && SCH.op[role][i].kind != K_PW;
  return c;
}
constexpr int vm_need(int role, int step) {
  int c = 0, need = 0;
  for (int i = 0; i < SCH.n[role]; ++i) {
    c += SCH.op[role][i].kind != K_PW;
    if (SCH.op[role][i].kind != K_PW && SCH.op[role][i].due <= step) need = c;
  }
  return need;
}
constexpr int vm_wait(int role, int step) {
  return vm_need(role, step) > vm_need(role, step - 1) ? vm_cnt(role, step - 1) - vm_need(role, step) : -1;
}
constexpr int op_first(int role, int step) {
  int i = 0;
  while (i < SCH.n[role] && SCH.op[role][i].step < step) ++i;
  return i;
}

template <int N> DI void wait_vm() { asm volatile("s_waitcnt vmcnt(%0)" ::"n"(N) : "memory"); }
DI void raw_barrier() {
  asm volatile("s_waitcnt lgkmcnt(0)" ::: "memory");
  __builtin_amdgcn_s_barrier();
  asm volatile("" ::: "memory");
}
DI void dma16(unsigned voff, const void* sbase, unsigned lds_dst) {
  unsigned keep;
  asm volatile("s_mov_b32 %0, m0\n\ts_mov_b32 m0, %3\n\ts_nop 0\n\tglobal_load_lds_dwordx4 %1, %2\n\ts_mov_b32 m0, %0"
               : "=&s"(keep) : "v"(voff), "s"(sbase), "s"(lds_dst) : "memory");
}
DI void dma16_nt(unsigned voff, const void* sbase, unsigned lds_dst) {
  unsigned keep;
  asm volatile("s_mov_b32 %0, m0\n\ts_mov_b32 m0, %3\n\ts_nop 0\n\tglobal_load_lds_dwordx4 %1, %2 nt\n\ts_mov_b32 m0, %0"
               : "=&s"(keep) : "v"(voff), "s"(sbase), "s"(lds_dst) : "memory");
}
template <int LO, int HI> DI void touch(const char* a) {
  asm volatile("s_mov_b32 exec_lo, %1\n\ts_mov_b32 exec_hi, %2\n\tglobal_load_dword v255, %0, off\n\ts_mov_b64 exec, -1" ::"v"(a), "n"(LO), "n"(HI) : "v255", "memory");
}
DI void dma16_48(unsigned voff, const void* sbase, unsigned lds_dst) {
  unsigned keep;
  asm volatile("s_mov_b32 %0, m0\n\ts_mov_b32 m0, %3\n\ts_mov_b32 exec_hi, 0xffff\n\tglobal_load_lds_dwordx4 %1, %2\n\ts_mov_b32 exec_hi, -1\n\ts_mov_b32 m0, %0"
               : "=&s"(keep) : "v"(voff), "s"(sbase), "s"(lds_dst) : "memory");
}
DI void gload_nt(u32x4& d, unsigned voff, const void* sbase) {
  asm volatile("global_load_dwordx4 %0, %1, %2 nt" : "=v"(d) : "v"(voff), "s"(sbase) : "memory");
}
DI void mfma_acc(f32x4& c, bf16x8 a, bf16x8 b) {
  asm volatile("v_mfma_f32_16x16x32_bf16 %0, %1, %2, %0" : "+v"(c) : "v"(a), "v"(b));
}
DI void mfma_init(f32x4& c, bf16x8 a, bf16x8 b) {
  asm volatile("v_mfma_f32_16x16x32_bf16 %0, %1, %2, 0" : "=&v"(c) : "v"(a), "v"(b));
}
DI int lane_id() {
  int l;
  asm volatile("v_mbcnt_lo_u32_b32 %0, -1, 0\n\tv_mbcnt_hi_u32_b32 %0, -1, %0" : "=v"(l));
  return l;
}
template <int V> struct IC { static constexpr int value = V; };
typedef const __attribute__((address_space(3))) bf16x8* lds_frag_ptr;
typedef __attribute__((address_space(3))) u32x4* lds_u32x4_ptr;
DI bf16x8 lds_frag(unsigned a) { return *(lds_frag_ptr)(uintptr_t)a; }

typedef const volatile __attribute__((address_space(3))) s16x4* lds_half_ptr;
DI s16x4 lds_half(unsigned a) { return *(lds_half_ptr)(uintptr_t)a; }
template <int N, class F> DI void unroll(F&& f) {
  if constexpr (N > 0) {
    unroll<N - 1>(f);
    f(IC<N - 1>{});
  }
}
constexpr int ops0(int n) {
  return (n + 3 <= 15 ? 8 : 0) + (n >= 2 && !(n & 1) ? 2 : 0) + (n == 3) + (n == 6 || n == 8 || n == 10) + (n == 12);
}
constexpr int after0(int n) { return n == 12; }

__global__ __launch_bounds__(512, 2) void fused_kernel(
    const short* __restrict__ xA, const short* __restrict__ kA, const short* __restrict__ vA,
    const short* __restrict__ wqA, const short* __restrict__ woA, const short* __restrict__ xB,
    const short* __restrict__ kB, const short* __restrict__ vB, const short* __restrict__ wqB,
    const short* __restrict__ woB, char* p_ws, unsigned short* __restrict__ out) {
  extern __shared__ __attribute__((aligned(16))) char smem[];
  const int wave = __builtin_amdgcn_readfirstlane(threadIdx.x >> 6);
  const int bid = blockIdx.x;
  const int xcd = bid & 7, idx = bid >> 3;
  const int blk = xcd >> 2;
  const int hg = (xcd & 3) * 2 + (idx & 1);
  const int b = idx >> 1;
  const int h0 = hg * 4;
  const int u = wave >> 1, qh = wave & 1;

  const char* __restrict__ x = (const char*)(blk ? xB : xA) + (size_t)b * SQ * DM * 2;
  const char* __restrict__ kc = (const char*)(blk ? kB : kA) + ((size_t)b * HQ + h0) * (SQ * DH * 2);
  const char* __restrict__ vc = (const char*)(blk ? vB : vA) + ((size_t)b * HQ + h0) * (SQ * DH * 2);
  const char* __restrict__ wq = (const char*)(blk ? wqB : wqA) + h0 * DH * 2;
  const char* __restrict__ wo = (const char*)(blk ? woB : woA) + (size_t)h0 * DH * DM * 2;

  const unsigned lds0 = (unsigned)(uintptr_t)smem;
  STAMP(0);

  const int w4 = wave & 3;
  const int grp = blk * NB + b;
  const char* lines2 = p_ws + WS_LINES + (size_t)(grp * 32 + w4) * 256;
  const char* src2 = p_ws + (size_t)(((grp * 8 + hg) * 8 * 4 + w4) * 3072);
  auto pc_of = [&](int p) { return (hg + 1 + p) & 7; };
  auto to_mask = [&](int n) { return ((((1u << n) - 1u) * 0x101u) >> ((7 - hg) & 7)) & 0xffu; };
  auto from_mask = [&](int n) { return ((((1u << n) - 1u) * 0x101u) >> ((n - hg) & 7)) & 0xffu; };
  auto front = [&](auto rc) __attribute__((always_inline)) {
    constexpr int ROLE = decltype(rc)::value;
    const int lane = lane_id();
    const int c = lane & 15, g = lane >> 4, qp = (lane & 15) >> 2, p = lane & 3;
    unsigned vw[2], vx[2], vk[2], vv[2];
#pragma unroll
    for (int e = 0; e < 2; ++e) {
      const int f = g | (e << 2);
      const int ch = (((c >> 1) ^ f) << 1) | (c & 1);
      vw[e] = (unsigned)(g * (HD * 2) + w4 * (DH * 2) + ch * 16);
      const int r = 24 * w4 + 8 * e + (lane >> 3);
      vx[e] = (unsigned)((24 * w4 + (lane >> 3)) * (DM * 2) + (((lane & 7) ^ ((r >> 1) & 7)) << 4));
      const int row = 8 * w4 + 4 * e + g;
      vk[e] = (unsigned)(row * 256 + ((c ^ (row & 15)) << 4));
    }

    auto issue_w = [&](int st, int e) {
      dma16(vw[(e >> 1) & 1], wq + (size_t)(st * 32 + 4 * e) * (HD * 2), lds0 + (st % 3) * 32768 + (w4 * 8 + e) * 1024);
    };
    auto issue_x = [&](int j, int e) {
      dma16(vx[e & 1], x + j * 128 + e * 8 * (DM * 2), lds0 + XBASE + (j & 1) * 12288 + (24 * w4 + 8 * e) * 128);
    };
    auto piece_src = [&](int i) { return (i >= 12 ? vc : kc) + piece_u(i) * (SQ * DH * 2) + piece_pc(i) * 8192; };
    auto piece_dst = [&](int i, int e) { return lds0 + piece_blk(i) * 8192 + (w4 * 2 + e) * 1024; };
    auto dma_piece = [&](int i, int e) { dma16_nt(i >= 12 ? vv[e] : vk[e], piece_src(i), piece_dst(i, e)); };

    const int fl = qp | ((g & 1) << 2);
    unsigned trw[8], trw2[8];
#pragma unroll
    for (int dt = 0; dt < 8; ++dt) {
      trw[dt] = lds0 + u * 8192 + (8 * g + qp) * 256 + 8 * p + ((dt ^ fl) << 5);
      trw2[dt] = trw[dt] + 65536;
      asm volatile("" : "+v"(trw2[dt]));
    }
    unsigned xrd[2];
#pragma unroll
    for (int hf = 0; hf < 2; ++hf)
      xrd[hf] = lds0 + XBASE + (qh * 48 + c) * 128 + (((4 * hf + g) ^ ((c >> 1) & 7)) << 4);
    auto read_a = [&](int st, int dt) {
      const unsigned a0 = st % 3 == 2 ? trw2[dt] : trw[dt] + (st % 3) * 32768;
      return cat8(tr_read(a0), tr_read(a0 + 4 * 256));
    };
    auto read_x = [&](int st, int t) { return lds_frag(xrd[st & 1] + ((st >> 1) & 1) * 12288 + t * 16 * 128); };

    f32x4 qacc[8][3];
    bf16x8 af[8], xf[3];
    u32x4 stg[20];
    unsigned seen = 0xffffffffu;
    const int tl = lane * 4 + w4;
    const char* ta = wq + (size_t)((tl >> 4) * 32 + 2 * b + ((tl >> 3) & 1)) * (HD * 2) + (tl & 7) * 128;

    auto do_op = [&](auto ic) {
      constexpr Op o = SCH.op[ROLE][decltype(ic)::value];
      if constexpr (o.kind == K_W) issue_w(o.a, o.b);
      if constexpr (o.kind == K_X) issue_x(o.a, o.b);
      if constexpr (o.kind == K_PD) dma_piece(o.a, o.b);
      if constexpr (o.kind == K_PR) gload_nt(stg[o.r], o.a >= 12 ? vv[o.b] : vk[o.b], piece_src(o.a));
      if constexpr (o.kind == K_PW) *(lds_u32x4_ptr)(uintptr_t)(piece_dst(o.a, o.b) + lane * 16) = stg[o.r];
      if constexpr (o.kind == K_T) {
        constexpr unsigned long long m = (o.b >= 16 ? ~0ull : (1ull << (4 * o.b)) - 1ull) << (4 * o.a);
        touch<(int)(unsigned)m, (int)(unsigned)(m >> 32)>(ta);
      }
      if constexpr (o.kind == K_SEEN)
        asm volatile("s_mov_b64 exec, 0xffff\n\tglobal_load_dword %0, %1, %2 sc1\n\ts_mov_b64 exec, -1"
                     : "+v"(seen) : "v"(lane * 16), "s"(lines2 + hg * 1024) : "memory");
      if constexpr (o.kind == K_PUB) {
        asm volatile("" : "+v"(seen));
        unsigned j = 0;
        while (__builtin_amdgcn_ballot_w64((seen >> 1) == j) != 0) ++j;
        asm volatile("s_mov_b64 exec, 3\n\tglobal_store_dword %0, %1, %2 sc1\n\ts_mov_b64 exec, -1"
                     ::"v"(lane * 128), "v"(2 * j), "s"(lines2 + hg * 1040) : "memory");
      }
    };
    auto mem_ops = [&](auto sc, auto slc) {
      constexpr int s = decltype(sc)::value, sl = decltype(slc)::value, f = op_first(ROLE, s);
      unroll<op_first(ROLE, s + 1) - f>([&](auto ic) {
        constexpr int i = f + decltype(ic)::value;
        if constexpr (SCH.op[ROLE][i].slot == sl) do_op(IC<i>{});
      });
    };

    mem_ops(IC<-1>{}, IC<0>{});
    wait_vm<0>();
    mem_ops(IC<-1>{}, IC<1>{});
    raw_barrier();
    STAMP(1);
    STAMPW(2);
#pragma unroll
    for (int t = 0; t < 3; ++t) xf[t] = read_x(0, t);
#pragma unroll
    for (int dt = 0; dt < 8; ++dt) af[dt] = read_a(0, dt);

    auto group = [&](auto sc, auto dc, bf16x8* an, bf16x8* xn) {
      constexpr int s = decltype(sc)::value, dt = decltype(dc)::value;
#pragma unroll
      for (int t = 0; t < 3; ++t) {
        if constexpr (s == 0) mfma_init(qacc[dt][t], af[dt], xf[t]);
        else mfma_acc(qacc[dt][t], af[dt], xf[t]);
      }
      if constexpr (s > 0) {
        if constexpr (dt < 7) an[dt] = read_a(s + 1, dt);
        if constexpr (dt >= 1 && dt <= 3) xn[dt - 1] = read_x(s + 1, dt - 1);
        if constexpr (dt == 6) an[7] = read_a(s + 1, 7);
      }
      mem_ops(sc, dc);
      __builtin_amdgcn_sched_barrier(0);
    };
    auto step = [&](auto ic) {
      constexpr int s = decltype(ic)::value;
      if constexpr (s > 0 && vm_wait(ROLE, s) >= 0) wait_vm<vm_wait(ROLE, s)>();
      raw_barrier();
      STAMP(3 + s);
      bf16x8 xn[3], an[8];
      mem_ops(ic, IC<-1>{});
      __builtin_amdgcn_sched_barrier(0);
      group(ic, IC<0>{}, an, xn); group(ic, IC<1>{}, an, xn); group(ic, IC<2>{}, an, xn); group(ic, IC<3>{}, an, xn);
      group(ic, IC<4>{}, an, xn); group(ic, IC<5>{}, an, xn); group(ic, IC<6>{}, an, xn); group(ic, IC<7>{}, an, xn);
      if constexpr (s == 0) {
        if constexpr (vm_wait(ROLE, 0) >= 0) wait_vm<vm_cnt(ROLE, 0) - vm_need(ROLE, 0)>();
        raw_barrier();
#pragma unroll
        for (int t = 0; t < 3; ++t) xn[t] = read_x(1, t);
#pragma unroll
        for (int dt = 0; dt < 8; ++dt) an[dt] = read_a(1, dt);
      }
#pragma unroll
      for (int t = 0; t < 3; ++t) xf[t] = xn[t];
#pragma unroll
      for (int dt = 0; dt < 8; ++dt) af[dt] = an[dt];
    };
    step(IC<0>{}); step(IC<1>{}); step(IC<2>{}); step(IC<3>{}); step(IC<4>{}); step(IC<5>{}); step(IC<6>{}); step(IC<7>{});
    step(IC<8>{}); step(IC<9>{}); step(IC<10>{}); step(IC<11>{}); step(IC<12>{});
    {
      const int l = lane_id();
#pragma unroll
      for (int e = 0; e < 2; ++e) {
        const int row = 8 * w4 + 4 * e + (l >> 4);
        vv[e] = (unsigned)(row * 256 + ((((((l & 15) >> 1) ^ (row & 7)) << 1) | (l & 1)) << 4));
      }
    }
    step(IC<13>{}); step(IC<14>{});

    if constexpr (vm_wait(ROLE, 15) >= 0) wait_vm<vm_wait(ROLE, 15)>();
    raw_barrier();
    STAMP(18);
    const int lane1 = lane_id();
    const int c1 = lane1 & 15, g1 = lane1 >> 4, qp1 = (lane1 & 15) >> 2, p1 = lane1 & 3;
    const unsigned l16 = (unsigned)lane1 * 16;
    u32x4 qf[4][3];
    bf16x8 kf[6][4];
    const unsigned kl = (unsigned)(c1 * 256 + (((g1 >> 1) ^ c1) << 4) + (g1 & 1) * 8);
    const unsigned kbase = lds0 + u * 8192;
    auto read_k = [&](auto jc) {
      constexpr int j = decltype(jc)::value;
      if constexpr (j < 24) {
        constexpr int s = j / 6, kt = j % 6;
        constexpr unsigned rb = kt < 2 ? 131072u : 32768u, off = (kt >= 4 ? 32768u : 0u) + (kt & 1) * 4096u;
        kf[kt][s] = cat8(lds_half((kl ^ (s << 6)) + (kbase + rb) + off), lds_half((kl ^ ((s << 6) | 32)) + (kbase + rb) + off));
      }
    };
    auto q_cvt = [&](int dt) {
#pragma unroll
      for (int t = 0; t < 3; ++t) {
        qf[dt >> 1][t][(dt & 1) * 2] = pack2(qacc[dt][t][0], qacc[dt][t][1]);
        qf[dt >> 1][t][(dt & 1) * 2 + 1] = pack2(qacc[dt][t][2], qacc[dt][t][3]);
      }
    };
    unroll<8>([&](auto dc) {
      constexpr int dt = decltype(dc)::value;
#pragma unroll
      for (int t = 0; t < 3; ++t) qacc[dt][t] = MFMA16(af[dt], xf[t], qacc[dt][t]);
      if constexpr (dt >= 2) q_cvt(dt - 2);
      if constexpr (dt == 7) q_cvt(6);
      read_k(dc);
      mem_ops(IC<15>{}, dc);
      __builtin_amdgcn_sched_barrier(0);
    });

    const f32x4 zero4 = {0.f, 0.f, 0.f, 0.f};
    const float cexp = 0.08838834764831845f * 1.4426950408889634f;
    f32x4 sacc[6][3], oacc[8][3], lsum[3];
    float mx[3], ex[3][24], rinv[3];
    u32x4 pf[3][3];
    bf16x8 vf[3][8], of[2][2][4][3];
    auto s_mm = [&](auto tc, auto jc) {
      constexpr int t = decltype(tc)::value, j = decltype(jc)::value, s = j / 6, kt = j % 6;
      sacc[kt][t] = MFMA16(kf[kt][s], __builtin_bit_cast(bf16x8, qf[s][t]), s == 0 ? zero4 : sacc[kt][t]);
    };
    auto m_op = [&](auto tc, auto qc) {
      constexpr int t = decltype(tc)::value, q = decltype(qc)::value;
      auto sv = [&](int j) { return sacc[j >> 2][t][j & 3]; };
      if constexpr (q < 4) {
#pragma unroll
        for (int i = 3 * q; i < 3 * q + 3; ++i) {
          if (i == 0) mx[t] = fmaxf(fmaxf(sv(0), sv(1)), sv(2));
          else if (i == 11) mx[t] = fmaxf(mx[t], sv(23));
          else mx[t] = fmaxf(fmaxf(mx[t], sv(2 * i + 1)), sv(2 * i + 2));
        }
      } else if constexpr (q == 4) {
        auto s = __builtin_amdgcn_permlane16_swap(__float_as_uint(mx[t]), __float_as_uint(mx[t]), false, false);
        mx[t] = fmaxf(__uint_as_float(s[0]), __uint_as_float(s[1]));
      } else if constexpr (q == 5) {
        auto s = __builtin_amdgcn_permlane32_swap(__float_as_uint(mx[t]), __float_as_uint(mx[t]), false, false);
        mx[t] = fmaxf(__uint_as_float(s[0]), __uint_as_float(s[1])) * -cexp;
      }
#pragma unroll
      for (int j2 = 0; j2 < 12; ++j2) {
        const int st = 6 + (j2 * 15) / 11;
        if (q == st + 2) pf[j2 >> 2][t][j2 & 3] = pack2(ex[t][2 * j2], ex[t][2 * j2 + 1]);
        if (q == st + 1) {
          ex[t][2 * j2] = __builtin_amdgcn_exp2f(ex[t][2 * j2]);
          ex[t][2 * j2 + 1] = __builtin_amdgcn_exp2f(ex[t][2 * j2 + 1]);
        }
        if (q == st) {
          ex[t][2 * j2] = fmaf(sv(2 * j2), cexp, mx[t]);
          ex[t][2 * j2 + 1] = fmaf(sv(2 * j2 + 1), cexp, mx[t]);
        }
      }
    };
    const int fl1 = qp1 | ((g1 & 1) << 2);
    const unsigned vl = (unsigned)((4 * g1 + qp1) * 256 + 8 * p1 + (fl1 << 5));
    auto read_v = [&](auto ic) {
      constexpr int i = decltype(ic)::value, s = i < 8 ? 0 : i < 16 ? 2 : 1, dt = i & 7;
      const unsigned a = (vl ^ (dt << 5)) + (kbase + (s == 1 ? 0u : 65536u)) + (s == 2 ? 32768u : 0u);
      vf[s][dt] = cat8(tr_read(a), tr_read(a + 16 * 256));
    };
    u32x4 ones = {0x3f803f80u, 0x3f803f80u, 0x3f803f80u, 0x3f803f80u};
    auto p_mm = [&](auto tc, auto jc) {
      constexpr int t = decltype(tc)::value, j = decltype(jc)::value, s = j < 9 ? 0 : j < 18 ? 2 : 1, dt = j % 9;
      const bf16x8 pb = __builtin_bit_cast(bf16x8, pf[s][t]);
      if constexpr (dt == 8) lsum[t] = MFMA16(__builtin_bit_cast(bf16x8, ones), pb, j < 9 ? zero4 : lsum[t]);
      else oacc[dt][t] = MFMA16(vf[s][dt], pb, j < 9 ? zero4 : oacc[dt][t]);
    };
    const unsigned ow = lds0 + wave * 4096 + l16;
    auto o_op = [&](auto tc, auto qc) {
      constexpr int t = decltype(tc)::value, q = decltype(qc)::value;
      if constexpr (q == 0) {
        rinv[t] = __builtin_amdgcn_rcpf(lsum[t][0]);
      } else if constexpr (q <= 12) {
        constexpr int s = (q - 1) / 3, k = (q - 1) % 3;
        if constexpr (k < 2) {
#pragma unroll
          for (int r = 0; r < 4; ++r) oacc[2 * s + k][t][r] *= rinv[t];
        } else {
          u32x4 r;
          r[0] = pack2(oacc[2 * s][t][0], oacc[2 * s][t][1]);
          r[1] = pack2(oacc[2 * s][t][2], oacc[2 * s][t][3]);
          r[2] = pack2(oacc[2 * s + 1][t][0], oacc[2 * s + 1][t][1]);
          r[3] = pack2(oacc[2 * s + 1][t][2], oacc[2 * s + 1][t][3]);
          *(lds_u32x4_ptr)(uintptr_t)(ow + o_reg(t) + s * 1024) = r;
          of[ROLE][0][s][t] = __builtin_bit_cast(bf16x8, r);
        }
      }
    };
    const unsigned ord0 = lds0 + qh * 4096 + (u & 1) * 8192 + l16;
    auto read_o = [&](auto tc, auto ic) {
      constexpr int t = decltype(tc)::value, i = decltype(ic)::value;
      constexpr int half = i < 4 ? ROLE : 1 - ROLE, hh = i < 4 ? 1 : (i - 4) >> 2, s = i & 3;
      of[half][hh][s][t] = lds_frag((ord0 ^ (hh * 8192)) + o_reg(t) + (half * 16384 + s * 1024));
    };
    unsigned vo;
    {
      const int srow = lane1 >> 3, pos = lane1 & 7;
      vo = (unsigned)((w4 * 64 + srow) * (DM * 2) + (((((pos >> 1) ^ ((srow >> 1) & 3)) << 1) | (pos & 1)) << 4));
    }
    auto issue_wo = [&](int st, int e) {
      dma16(vo, wo + (size_t)((st & 1) * 256 + e * 8) * (DM * 2) + pc_of(st >> 1) * 128, lds0 + st_reg(st) + (w4 * 64 + e * 8) * 128);
    };
    const char* lines = lines2 + ROLE * 128;
    const char* src = src2 + ROLE * 1536;
    const unsigned others = 0xffu & ~(1u << hg);
    unsigned tag = 0, mk = 0, pl = 0;
    auto signal = [&](unsigned v, unsigned m) {
      unsigned a;
      asm volatile("v_lshlrev_b32 %0, 6, %1\n\ts_mov_b32 exec_lo, %4\n\ts_mov_b32 exec_hi, 0\n\tglobal_store_dword %0, %2, %3 sc1\n\ts_mov_b64 exec, -1"
                   : "=&v"(a) : "v"(l16), "v"(v), "s"(lines + hg * 16), "s"(m) : "memory");
    };
    auto poll = [&](unsigned& f) {
      asm volatile("s_mov_b64 exec, 0xff\n\tglobal_load_dword %0, %1, %2 sc1\n\ts_mov_b64 exec, -1"
                   : "+v"(f) : "v"(l16), "s"(lines + hg * 1024) : "memory");
    };
    auto marks = [&](unsigned& m) {
      unsigned a;
      asm volatile("v_mul_u32_u24 %1, %4, %2\n\ts_mov_b64 exec, 0xff\n\tglobal_load_dword %0, %1, %3 sc1\n\ts_mov_b64 exec, -1"
                   : "+v"(m), "=&v"(a) : "v"(l16), "s"(lines), "n"(65) : "memory");
    };
    auto heard = [&](unsigned& f) {
      asm volatile("" : "+v"(f));
      const unsigned got = (unsigned)__builtin_amdgcn_ballot_w64((f ^ __builtin_amdgcn_readlane(f, hg)) < 2u);
      return (got & 0xffu) == 0xffu;
    };
    auto ready = [&](unsigned& f, unsigned need) {
      asm volatile("" : "+v"(f));
      const unsigned got = (unsigned)__builtin_amdgcn_ballot_w64(f == __builtin_amdgcn_readlane(f, hg) + 1u);
      return (got & need) == need;
    };
    auto retag = [&](unsigned& m) {
      asm volatile("" : "+v"(m));
      const unsigned bad = (unsigned)__builtin_amdgcn_ballot_w64(m != tag) & others;
      tag = m;
      if (bad) signal(tag, bad);
    };

    unroll<24>([&](auto jc) {
      constexpr int j = decltype(jc)::value;
      s_mm(IC<0>{}, jc);
      if constexpr (j == 1) q_cvt(7);
      read_k(IC<j + 8>{});
      __builtin_amdgcn_sched_barrier(0);
    });
    wait_vm<8>();
    raw_barrier();
    STAMP(19);
    unroll<24>([&](auto jc) {
      constexpr int j = decltype(jc)::value;
      s_mm(IC<1>{}, jc);
      if constexpr (j < 4) *(lds_u32x4_ptr)(uintptr_t)(piece_dst(12 + 2 * ROLE + (j >> 1), j & 1) + l16) = stg[16 + j];
      if constexpr (ROLE == 1 && j == 4) marks(tag);
      if constexpr (j >= 6 && j < 16 && !(j & 1) && (j - 6) / 2 * 2 + ROLE < 9) {
        constexpr int q = (j - 6) / 2 * 2 + ROLE;
        asm volatile("global_store_dwordx4 %0, %1, %2 offset:%3 sc1\n\ts_nop 1" ::"v"(l16), "v"((u32x4){EMPTY, EMPTY, EMPTY, EMPTY}),
                     "s"(src2 + (5 + q / 3) * 12288), "n"((q % 3) * 1024) : "memory");
      }
      if constexpr (j == 17) {
        const int tw = (lane1 & 31) * 8 + wave;
        touch<-1, 0>(wo + ((tw >> 4) * 32 + 2 * b + ((tw >> 3) & 1)) * (DM * 2) + (tw & 7) * 128);
      }
      m_op(IC<0>{}, jc);
      __builtin_amdgcn_sched_barrier(0);
    });
    raw_barrier();
    STAMP(20);
    unroll<24>([&](auto jc) {
      constexpr int j = decltype(jc)::value;
      s_mm(IC<2>{}, jc);
      m_op(IC<1>{}, jc);
      if constexpr (j >= 8 && j < 24 && !(j & 1)) read_v(IC<(j - 8) / 2>{});
      __builtin_amdgcn_sched_barrier(0);
    });
    wait_vm<1>();
    raw_barrier();
    STAMP(21);
    unroll<27>([&](auto jc) {
      constexpr int j = decltype(jc)::value;
      p_mm(IC<0>{}, jc);
      if constexpr (j < 16) read_v(IC<j + 8>{});
      if constexpr (ROLE == 1 && j == 0) { asm volatile("" : "+v"(tag)); signal(tag, others); }
      if constexpr (j >= 2 && j < 26) m_op(IC<2>{}, IC<j - 2>{});
      if constexpr (j % 3 == 1 && j < 24) issue_wo(ROLE, j / 3);
      __builtin_amdgcn_sched_barrier(0);
    });
    raw_barrier();
    STAMP(22);
    unroll<27>([&](auto jc) {
      constexpr int j = decltype(jc)::value;
      p_mm(IC<1>{}, jc);
      if constexpr (j & 1) o_op(IC<0>{}, IC<j / 2>{});
      __builtin_amdgcn_sched_barrier(0);
    });
    raw_barrier();
    STAMP(23);
    unroll<27>([&](auto jc) {
      constexpr int j = decltype(jc)::value;
      p_mm(IC<2>{}, jc);
      if constexpr (j & 1) o_op(IC<1>{}, IC<j / 2>{});
      if constexpr (j >= 2 && j < 26 && !(j & 1)) read_o(IC<0>{}, IC<(j - 2) / 2>{});
      __builtin_amdgcn_sched_barrier(0);
    });
    wait_vm<0>();
    raw_barrier();
    STAMP(24);

    auto sidx = [](int n) { return n < 2 ? n ^ ROLE : n; };
    unsigned wa[2];
    wa[0] = lds0 + st_reg(sidx(0)) + (u & 1) * 16384 + (4 * g1 + qp1) * 128 + 8 * p1 + ((u ^ ((2 * g1 + (qp1 >> 1)) & 3)) << 5);
    wa[1] = wa[0] ^ 16384u;
    asm volatile("" : "+v"(wa[0]), "+v"(wa[1]));
    bf16x8 wf[4];
    auto read_wo = [&](auto fc) {
      constexpr int f = decltype(fc)::value, n = f >> 3, hh = (f >> 2) & 1, ks = f & 3;
      if constexpr (n < 16) {
        if constexpr (n > 0 && ks == 0)
          asm volatile("v_add_u32 %0, %1, %0" : "+v"(wa[hh]) : "n"(st_reg(n < 2 ? n ^ ROLE : n) - st_reg(n - 1 < 2 ? (n - 1) ^ ROLE : n - 1)));
        wf[f & 3] = cat8(tr_read(wa[hh] + ks * 4096), tr_read(wa[hh] + ks * 4096 + 16 * 128));
      }
    };
    unroll<13>([&](auto jc) {
      constexpr int j = decltype(jc)::value;
      if constexpr (j < 12) read_o(IC<1>{}, jc);
      o_op(IC<2>{}, jc);
      if constexpr (ROLE == 0 && j < 8) issue_wo(2, j);
      if constexpr (j >= 10) read_wo(IC<j - 10>{});
      __builtin_amdgcn_sched_barrier(0);
    });
    raw_barrier();
    STAMP(25);

    f32x4 acc[2][3];
    u32x4 in4[7];
    u32x2 in2[7];
    unsigned early = 0;
    const unsigned l8 = l16 >> 1;
    auto load_in = [&](int k) {
      asm volatile("global_load_dwordx4 %0, %2, %4 sc1\n\tglobal_load_dwordx2 %1, %3, %4 offset:1024 sc1"
                   : "=&v"(in4[k - 1]), "=&v"(in2[k - 1]) : "v"(l16), "v"(l8), "s"(src + k * 12288) : "memory");
    };
    auto oint = [&](auto nc) {
      constexpr int n = decltype(nc)::value;
      constexpr int half = (n < 2 ? n ^ ROLE : n) & 1, pb = (n >> 1) & 1;
      unroll<24>([&](auto ic) {
        constexpr int i = decltype(ic)::value, f = i / 3, t = i % 3;
        if constexpr (!(n & 1) && f == 0) mfma_init(acc[pb][t], wf[f & 3], of[half][f >> 2][f & 3][t]);
        else mfma_acc(acc[pb][t], wf[f & 3], of[half][f >> 2][f & 3][t]);
        if constexpr (t == 0) read_wo(IC<n * 8 + f + 3>{});
        if constexpr (n == 0 && i < 12) read_o(IC<2>{}, ic);
        if constexpr (ROLE == 0 && t == 0 && n + 3 <= 15) issue_wo(n + 3, f);
        if constexpr (ROLE == 1 && n == 0 && i == 1) poll(pl);
        if constexpr (n == 3 && i == 1) marks(mk);
        if constexpr ((n == 6 || n == 8 || n == 10) && i == 1) {
          if constexpr (ROLE == 1) wait_vm<0>();
          if constexpr (n == 6) {
            if constexpr (ROLE == 1) retag(mk);
            asm volatile("" : "+v"(mk));
            tag = mk + 1u;
          }
          signal(tag, n == 6 ? to_mask(2) : 1u << pc_of(n / 2 - 2));
        }
        if constexpr (n >= 2 && !(n & 1) && i == 4) {
          constexpr int p = n / 2 - 1;
          const char* dst = p_ws + (size_t)((((grp * 8 + pc_of(p)) * 8 + p + 1) * 4 + w4) * 3072 + ROLE * 1536);
          u32x4 r;
          u32x2 r2;
          r[0] = pack2(acc[1 - pb][0][0], acc[1 - pb][0][1]); r[1] = pack2(acc[1 - pb][0][2], acc[1 - pb][0][3]);
          r[2] = pack2(acc[1 - pb][1][0], acc[1 - pb][1][1]); r[3] = pack2(acc[1 - pb][1][2], acc[1 - pb][1][3]);
          r2[0] = pack2(acc[1 - pb][2][0], acc[1 - pb][2][1]); r2[1] = pack2(acc[1 - pb][2][2], acc[1 - pb][2][3]);
          asm volatile("global_store_dwordx4 %0, %2, %4 sc1\n\tglobal_store_dwordx2 %1, %3, %4 offset:1024 sc1\n\ts_nop 1"
                       ::"v"(l16), "v"(l8), "v"(r), "v"(r2), "s"(dst) : "memory");
        }
        if constexpr (n == 12 && i == 23) poll(pl);
        if constexpr (n == 15 && i == 0) {
          wait_vm<2>();
          early = __builtin_amdgcn_readfirstlane(ready(pl, from_mask(4)) ? 1u : 0u);
        }
        if constexpr (n == 15 && i >= 1 && i <= 6) load_in(i);
        if constexpr (n == 15 && i == 16) load_in(7);
        __builtin_amdgcn_sched_barrier(0);
      });
      if constexpr (ROLE == 1 && n == 2) {
        wait_vm<2>();
        if (!heard(pl)) {
#pragma unroll 1
          for (int it = 0; it < SPIN_CAP; ++it) {
            unsigned m = tag;
            poll(pl);
            marks(m);
            wait_vm<0>();
            retag(m);
            if (heard(pl)) break;
            __builtin_amdgcn_s_sleep(1);
          }
        }
      }
      if constexpr (n < 14) {
        if constexpr (ROLE == 0) wait_vm<ops0(n) + (n > 0 ? after0(n - 1) : 0)>();
        raw_barrier();
        if constexpr (n & 1) { STAMP(26 + n / 2); }
      }
    };
    unroll<16>(oint);
    asm volatile("s_nop 7\n\ts_nop 3" ::: "memory");
    STAMPW(33);

    auto add_in = [&](int k) {
      asm volatile("" : "+v"(in4[k - 1]), "+v"(in2[k - 1]));
#pragma unroll
      for (int e = 0; e < 6; ++e) {
        const unsigned w = e < 4 ? in4[k - 1][e & 3] : in2[k - 1][e & 1];
        acc[1][e >> 1][2 * (e & 1)] += __uint_as_float(w << 16);
        acc[1][e >> 1][2 * (e & 1) + 1] += __uint_as_float(w & 0xffff0000u);
      }
      asm volatile("" : "+v"(acc[1][0]), "+v"(acc[1][1]), "+v"(acc[1][2]));
    };
    auto whole = [&]() {
      bool ok = true;
#pragma unroll
      for (int k = 4; k < 7; ++k) {
        asm volatile("" : "+v"(in4[k]), "+v"(in2[k]));
        ok = ok && in4[k][0] != EMPTY && in2[k][0] != EMPTY;
      }
      return __builtin_amdgcn_ballot_w64(!ok) == 0;
    };
    wait_vm<6>();
    if (!early) {
#pragma unroll 1
      for (int it = 0; it < SPIN_CAP; ++it) {
        poll(pl);
        wait_vm<0>();
        if (ready(pl, from_mask(4))) break;
        __builtin_amdgcn_s_sleep(1);
      }
      load_in(1); load_in(2); load_in(3); load_in(4);
      wait_vm<0>();
    }
    add_in(1); add_in(2); add_in(3); add_in(4);
    STAMPW(34);
    wait_vm<0>();
    if (!whole()) {
#pragma unroll 1
      for (int it = 0; it < SPIN_CAP; ++it) {
        load_in(5); load_in(6); load_in(7);
        wait_vm<0>();
        if (whole()) break;
      }
    }
    add_in(5); add_in(6); add_in(7);
    STAMPW(35);
    {
      const int lane2 = lane_id();
      unsigned short* o = out + (size_t)(b * SQ + qh * 48 + (lane2 & 15)) * 1024 + blk * 512 + hg * 64 + u * 16 + 4 * (lane2 >> 4);
#pragma unroll
      for (int t = 0; t < 3; ++t) {
        u32x2 r;
        r[0] = pack2(acc[1][t][0], acc[1][t][1]);
        r[1] = pack2(acc[1][t][2], acc[1][t][3]);
        *(u32x2*)(o + t * 16 * 1024) = r;
      }
    }
    STAMPW(36);
    STAMPEND(37);
  };
  if (wave >= 4) {
    front(IC<1>{});
    __builtin_amdgcn_endpgm();
  }
  front(IC<0>{});
}

}

extern "C" void kernel_launch(void* const* d_in, const int* in_sizes, int n_in, void* d_out, int out_size,
                              void* d_ws, size_t ws_size, hipStream_t stream) {
  const short* xA = (const short*)d_in[0];
  const short* kA = (const short*)d_in[1];
  const short* vA = (const short*)d_in[2];
  const short* wqA = (const short*)d_in[3];
  const short* woA = (const short*)d_in[4];
  const short* xB = (const short*)d_in[5];
  const short* kB = (const short*)d_in[6];
  const short* vB = (const short*)d_in[7];
  const short* wqB = (const short*)d_in[8];
  const short* woB = (const short*)d_in[9];
  (void)hipFuncSetAttribute((const void*)fused_kernel, hipFuncAttributeMaxDynamicSharedMemorySize, K1_LDS);
  hipLaunchKernelGGL(fused_kernel, dim3(256), dim3(512), K1_LDS, stream, xA, kA, vA, wqA, woA, xB, kB, vB, wqB,
                     woB, (char*)d_ws, (unsigned short*)d_out);
}
```

```cpp
#include <hip/hip_runtime.h>
#include <hip/hip_bf16.h>
#include <stdint.h>

typedef __attribute__((ext_vector_type(8))) short bf16x8;
typedef __attribute__((ext_vector_type(4))) short s16x4;
typedef __attribute__((ext_vector_type(4))) float f32x4;
typedef __attribute__((ext_vector_type(2))) float f32x2;
typedef __attribute__((ext_vector_type(4))) unsigned u32x4;
typedef __attribute__((ext_vector_type(2))) unsigned u32x2;
typedef __attribute__((ext_vector_type(2))) __bf16 bf16x2_t;
typedef __attribute__((address_space(3))) s16x4* lds_s16x4_ptr;

#define XT(k)
#define XF(k)
#define DI __device__ __forceinline__
#define MFMA16(a, b, c) __builtin_amdgcn_mfma_f32_16x16x32_bf16((a), (b), (c), 0, 0, 0)

#define XS(k)
#define STAMP(i)
#define STAMPW(i)
#define STAMPEND(i)
namespace {

constexpr int NB = 16, SQ = 96, DM = 512, HQ = 32, DH = 128, HD = HQ * DH;

DI unsigned pack2(float a, float b) {
  f32x2 v = {a, b};
  return __builtin_bit_cast(unsigned, __builtin_convertvector(v, bf16x2_t));
}
DI s16x4 tr_read(unsigned lds_addr) {
  return __builtin_amdgcn_ds_read_tr16_b64_v4i16((lds_s16x4_ptr)(uintptr_t)lds_addr);
}
DI bf16x8 cat8(s16x4 lo, s16x4 hi) { return __builtin_shufflevector(lo, hi, 0, 1, 2, 3, 4, 5, 6, 7); }
DI float xg_max(float x) {
  auto s = __builtin_amdgcn_permlane16_swap(__float_as_uint(x), __float_as_uint(x), false, false);
  x = fmaxf(__uint_as_float(s[0]), __uint_as_float(s[1]));
  auto t = __builtin_amdgcn_permlane32_swap(__float_as_uint(x), __float_as_uint(x), false, false);
  return fmaxf(__uint_as_float(t[0]), __uint_as_float(t[1]));
}
DI float xg_sum(float x) {
  auto s = __builtin_amdgcn_permlane16_swap(__float_as_uint(x), __float_as_uint(x), false, false);
  x = __uint_as_float(s[0]) + __uint_as_float(s[1]);
  auto t = __builtin_amdgcn_permlane32_swap(__float_as_uint(x), __float_as_uint(x), false, false);
  return __uint_as_float(t[0]) + __uint_as_float(t[1]);
}

constexpr int K1_LDS = 160 * 1024;
constexpr int XBASE = 128 * 1024;

constexpr int piece_u(int i) { return i & 3; }
constexpr int piece_pc(int i) { return (i >> 2) % 3; }
constexpr int piece_blk(int i) { return i < 12 ? i : i - 4; }
constexpr unsigned o_reg(int t) { return t == 0 ? 98304u : t == 1 ? 65536u : 131072u; }
constexpr unsigned st_reg(int st) {
  constexpr unsigned r[5] = {32768u, 0u, 98304u, 65536u, 131072u};
  return r[st % 5];
}
constexpr unsigned EMPTY = 0x7f817f81u;
constexpr unsigned WS_LINES = 32u << 20;
constexpr int SPIN_CAP = 1 << 16;

enum { K_W, K_X, K_PD, K_PR, K_PW, K_T, K_TX, K_TO, K_SEEN, K_PUB, K_XR, K_XW };
struct Op { int step, slot, kind, a, b, r, due, ph; };
struct Sched {
  Op op[2][400];
  int n[2];
  int ph;
  constexpr void add(int role, int step, int slot, int kind, int a, int b, int r, int due) {
    op[role][n[role]] = Op{step, slot, kind, a, b, r, due, ph};
    ++n[role];
  }
};
#define TOP 2
#define TOJ 0
#define STP 5
#define STJ 8
#define CLR 6
#define CLJ 2
#define NPH 3
#define BF 1
#define BP 3
#define NBU 3
#define NPP 0
#define NE 6
#define BSL 5
#define TAH 1
#define PRO 1
#define ROT 1
#define XR 0
#define DD 4
#define PST 1
#define CT 1
#define SPT 3
#define WT 0
#define TOUT 1
constexpr bool burst(int s, int ph) { return s == -1 || (s >= BF + ph * PST && (s - BF - ph * PST) % BP == 0 && s <= 11); }
constexpr int next_burst(int s, int ph) { for (int i = s + 1; i <= 11; ++i) if (burst(i, ph)) return i; return 99; }
constexpr Sched make_sched() {
  Sched S{};
  S.ph = -1;
  for (int e = 0; e < 3; ++e) S.add(0, -1, 0, K_X, 0, e, 0, -1);
  for (int e = 0; e < 8; ++e) S.add(0, -1, 0, K_W, 0, e, 0, -1);
  S.add(1, -1, 0, K_T, 1, 4, 0, -1);
  if (XR) for (int e = 0; e < 3; ++e) S.add(1, -1, 0, K_X, 1, e, 0, -1);
  if (PRO >= 2) S.add(1, -1, 0, K_TX, 1, 1, 0, -1);
  for (int e = 8; e < (PRO ? 16 : 24); ++e) S.add(1, -1, e < 16 ? 0 : 1, K_W, e >> 3, e & 7, 0, (e >> 3) - 1);
  if (!PRO) {
    for (int e = 0; e < 8; ++e) S.add(0, -1, 1, K_W, 3, e, 0, 2);
    if (!XR) for (int e = 0; e < 3; ++e) S.add(1, -1, 1, K_X, 1, e, 0, 1);
  }
  S.add(1, -1, 1, K_SEEN, 0, 0, 0, 3);
  int kp[NPH][2] = {}, tw[NPH] = {}, tx[NPH] = {}, nb[NPH] = {};
  auto piece = [&](int r, int s, int i) {
    const int pi = i < 8 ? (i >> 1) * 4 + 2 * r + (i & 1) : 21 + r;
    for (int e = 0; e < 2; ++e) S.add(r, s, 0, K_PR, pi, e, 2 * i + e, i < 6 ? 12 + (i >> 1) : 16);
  };
  auto slow = [&](int s, int ph) {
    const int f[2] = {S.n[0], S.n[1]};
    S.ph = ph;
    const int upto = next_burst(s, ph) + TAH;
    const int w1 = upto + 4 < 15 ? upto + 4 : 15, w0 = 5 + tw[ph];
    if (w0 <= w1) { S.add(1, s, 0, K_T, w0, w1 + 1 - w0, 0, 13); tw[ph] = w1 - 4; }
    const int x1 = (upto + 3) / 2 < 7 ? (upto + 3) / 2 : 7;
    for (int j = 0; XR && (j = 2 + tx[ph]) <= 7 && 2 * j - 7 <= s; ++tx[ph])
      for (int e = 0; e < 3; ++e) S.add(j & 1, s, 0, K_XR, j, e, 0, 2 * j - 3);
    while (!XR && 2 + tx[ph] <= x1) {
      const int x0 = 2 + tx[ph], e = (x0 | 3) < x1 ? (x0 | 3) : x1;
      S.add(1, s, 0, K_TX, x0, e + 1 - x0, 0, 13);
      tx[ph] = e - 1;
    }
    for (int r = 0; r < 2; ++r) {
      const int left = NBU + 1 - nb[ph];
      const int c = s < 0 ? NPP : left > 0 ? (NE - kp[ph][r] + left - 1 - ((nb[ph] + r) & 1) * (left > 1)) / left : 0;
      for (int i = 0; i < c; ++i) piece(r, s, kp[ph][r]++);
    }
    if (TOUT && nb[ph] == 1) S.add(1, s, 0, K_TO, 1, 0, 0, 13);
    if (s >= 3 && (nb[ph] == 1 || s - BP < 3)) S.add(1, s, 0, K_PUB, 0, 0, 0, 13);
    ++nb[ph];
    S.ph = -1;
    for (int r = 0; r < 2; ++r)
      for (int i = f[r]; i < S.n[r]; ++i) S.op[r][i].slot = s < 0 ? 1 : BSL + (i - f[r]) * (8 - BSL) / (S.n[r] - f[r]);
  };
  for (int s = -1; s < 16; ++s) {
    const int g[2] = {S.n[0], S.n[1]};
    if (PRO && s == 0) {
      if (!XR) for (int e = 0; e < 3; ++e) S.add(1, s, 0, K_X, 1, e, 0, 1);
      for (int e = 0; e < 8; ++e) S.add(0, s, 0, K_W, 2, e, 0, 1);
      for (int e = 0; e < 8; ++e) S.add(1, s, 0, K_W, 3, e, 0, 2);
    }
    if (PRO && DD == 4 && s == 2) for (int e = 0; e < 8; ++e) S.add(1, s, 0, K_W, 5, e, 0, 4);
    if (PRO && s == 1) for (int e = 0; e < 8; ++e) S.add(0, s, 0, K_W, 4, e, 0, 3);
    if (s >= (PRO ? 2 : 0) && s + DD <= 15) for (int e = 0; e < 8; ++e) S.add(0, s, 0, K_W, s + DD, e, 0, s + DD - 1);
    if (s > 0 && (s & 1) && (s + 3) / 2 <= 7) for (int e = 0; e < 3; ++e) S.add(XR ? (s + 3) / 2 & 1 : 1, s, 0, XR ? K_XW : K_X, (s + 3) / 2, e, 0, s + 2);
    for (int r = 0; r < 2; ++r) {
      if (s == 12) for (int i = NE; i < 8; ++i) piece(r, s, i);
      if (s >= 12 && s <= 14) for (int e = 0; e < 4; ++e) S.add(r, s, 0, K_PW, (s - 12) * 4 + 2 * r + (e >> 1), e & 1, (s - 12) * 4 + e, 99);
      if (s == 12) piece(r, s, 8);
      if (s == 13) for (int e = 0; e < 2; ++e) S.add(r, s, 0, K_PD, r ? 20 : 23, e, 0, 99);
      if (s == 15) for (int e = 0; e < 4; ++e) S.add(r, s, 0, K_PD, 16 + 2 * r + (e >> 1), e & 1, 0, 99);
    }
    const int h[2] = {S.n[0], S.n[1]};
    for (int ph = 0; ph < NPH; ++ph) if (burst(s, ph)) slow(s, ph);
    for (int r = 0; r < 2; ++r) {
      const int w = S.n[r] > h[r] ? BSL : 8, c = h[r] - g[r];
      for (int i = g[r]; i < h[r]; ++i) S.op[r][i].slot = c <= w ? i - g[r] : (i - g[r]) * w / c;
    }
  }
  return S;
}
constexpr Sched SCH = make_sched();
constexpr bool is_vm(const Op& o, int ph) { return o.kind != K_PW && o.kind != K_XW && (o.ph < 0 || o.ph == ph); }
constexpr int vm_cnt(int role, int step, int ph) {
  int c = 0;
  for (int i = 0; i < SCH.n[role]; ++i) c += SCH.op[role][i].step <= step && is_vm(SCH.op[role][i], ph);
  return c;
}
constexpr int vm_need(int role, int step, int ph) {
  int c = 0, need = 0;
  for (int i = 0; i < SCH.n[role]; ++i) {
    c += is_vm(SCH.op[role][i], ph);
    if (is_vm(SCH.op[role][i], ph) && SCH.op[role][i].due <= step) need = c;
  }
  return need;
}
constexpr int vm_wait(int role, int step, int ph) {
  return vm_need(role, step, ph) > vm_need(role, step - 1, ph) ? vm_cnt(role, step > 0 ? step - 1 : 0, ph) - vm_need(role, step, ph) : 63;
}
constexpr int op_first(int role, int step) {
  int i = 0;
  while (i < SCH.n[role] && SCH.op[role][i].step < step) ++i;
  return i;
}
constexpr bool has_ops(int role, int step, int slot, int ph) {
  for (int i = op_first(role, step); i < op_first(role, step + 1); ++i)
    if (SCH.op[role][i].slot == slot && SCH.op[role][i].ph == ph) return true;
  return false;
}

template <int N> DI void wait_vm() { asm volatile("s_waitcnt vmcnt(%0)" ::"n"(N) : "memory"); }
DI void raw_barrier() {
  asm volatile("s_waitcnt lgkmcnt(0)" ::: "memory");
  __builtin_amdgcn_s_barrier();
  asm volatile("" ::: "memory");
}
DI void dma16(unsigned voff, const void* sbase, unsigned lds_dst) {
  asm volatile("s_nop 0\n\tglobal_load_lds_dwordx4 %0, %1" ::"v"(voff), "s"(sbase), "{m0}"(lds_dst) : "memory");
}
DI void dma16_nt(unsigned voff, const void* sbase, unsigned lds_dst) {
  asm volatile("s_nop 0\n\tglobal_load_lds_dwordx4 %0, %1 nt" ::"v"(voff), "s"(sbase), "{m0}"(lds_dst) : "memory");
}
template <unsigned long long M> DI void touch(unsigned voff, const void* sbase) {
  asm volatile("s_mov_b32 exec_lo, %2\n\ts_mov_b32 exec_hi, %3\n\tglobal_load_dword v255, %0, %1\n\ts_mov_b64 exec, -1"
               ::"v"(voff), "s"(sbase), "n"((int)(unsigned)M), "n"((int)(unsigned)(M >> 32)) : "v255", "memory");
}
template <int OFF> DI void store_empty(unsigned voff, const void* sbase) {
  asm volatile("global_store_dwordx4 %0, %1, %2 offset:%3 sc1\n\ts_nop 1" ::"v"(voff), "v"((u32x4){EMPTY, EMPTY, EMPTY, EMPTY}),
               "s"(sbase), "n"(OFF) : "memory");
}
DI void gload_nt(u32x4& d, unsigned voff, const void* sbase) {
  asm volatile("global_load_dwordx4 %0, %1, %2 nt" : "=v"(d) : "v"(voff), "s"(sbase) : "memory");
}
DI void gload_keep(u32x4& d, unsigned voff, const void* sbase) {
  asm volatile("global_load_dwordx4 %0, %1, %2" : "+v"(d) : "v"(voff), "s"(sbase) : "memory");
}
DI void gload_nt_keep(u32x4& d, unsigned voff, const void* sbase) {
  asm volatile("global_load_dwordx4 %0, %1, %2 nt" : "+v"(d) : "v"(voff), "s"(sbase) : "memory");
}
DI void mfma_acc(f32x4& c, bf16x8 a, bf16x8 b) {
  asm volatile("v_mfma_f32_16x16x32_bf16 %0, %1, %2, %0" : "+v"(c) : "v"(a), "v"(b));
}
DI void mfma_init(f32x4& c, bf16x8 a, bf16x8 b) {
  asm volatile("v_mfma_f32_16x16x32_bf16 %0, %1, %2, 0" : "=&v"(c) : "v"(a), "v"(b));
}
DI int lane_id() {
  int l;
  asm volatile("v_mbcnt_lo_u32_b32 %0, -1, 0\n\tv_mbcnt_hi_u32_b32 %0, -1, %0" : "=v"(l));
  return l;
}
DI unsigned xbase(unsigned a, unsigned c) {
  a ^= c;
  asm volatile("" : "+v"(a));
  return a;
}
DI float max3(float a, float b, float c) {
  float r;
  asm("v_max3_f32 %0, %1, %2, %3" : "=v"(r) : "v"(a), "v"(b), "v"(c));
  return r;
}
DI float max2(float a, float b) {
  float r;
  asm("v_max_f32 %0, %1, %2" : "=v"(r) : "v"(a), "v"(b));
  return r;
}
DI float mul1(float a, float b) {
  float r;
  asm("v_mul_f32 %0, %1, %2" : "=v"(r) : "v"(a), "v"(b));
  return r;
}
template <int V> struct IC { static constexpr int value = V; };
typedef const __attribute__((address_space(3))) bf16x8* lds_frag_ptr;
typedef __attribute__((address_space(3))) u32x4* lds_u32x4_ptr;
DI bf16x8 lds_frag(unsigned a) { return *(lds_frag_ptr)(uintptr_t)a; }

template <int R> DI void code_end() { asm volatile(".Lcode_to%0:" ::"n"(R)); }
DI unsigned speek(const void* p) {
  unsigned r;
  asm volatile("s_load_dword %0, %1, 0x0" : "=s"(r) : "s"(p) : "memory");
  return r;
}
DI void speek_done(const unsigned* r) { asm volatile("s_waitcnt lgkmcnt(0)" ::"s"(r[0]), "s"(r[1]), "s"(r[2]), "s"(r[3]) : "memory"); }
template <unsigned long long M> DI void touch_dev(unsigned voff, const void* sbase) {
  asm volatile("s_mov_b32 exec_lo, %2\n\ts_mov_b32 exec_hi, %3\n\tglobal_load_dword v255, %0, %1 sc1\n\ts_mov_b64 exec, -1"
               ::"v"(voff), "s"(sbase), "n"((int)(unsigned)M), "n"((int)(unsigned)(M >> 32)) : "v255", "memory");
}
DI const char* pick(bool c, const char* a, const char* b) {
  const uintptr_t m = (uintptr_t)0 - (uintptr_t)c;
  return (const char*)((uintptr_t)b ^ (((uintptr_t)a ^ (uintptr_t)b) & m));
}
typedef const volatile __attribute__((address_space(3))) s16x4* lds_half_ptr;
DI s16x4 lds_half(unsigned a) { return *(lds_half_ptr)(uintptr_t)a; }
template <int N, class F> DI void unroll(F&& f) {
  if constexpr (N > 0) {
    unroll<N - 1>(f);
    f(IC<N - 1>{});
  }
}
constexpr int ops0(int n) {
  return (n + 3 <= 15 ? 8 : 0) + (n >= 2 && !(n & 1) ? 2 : 0) + (n == 3) + (n == 6 || n == 8 || n == 10) + (n == 12);
}
constexpr int after0(int n) { return n == 12; }

__global__ __launch_bounds__(512, 2) void fused_kernel(
    const short* __restrict__ xA, const short* __restrict__ kA, const short* __restrict__ vA,
    const short* __restrict__ wqA, const short* __restrict__ woA, const short* __restrict__ xB,
    const short* __restrict__ kB, const short* __restrict__ vB, const short* __restrict__ wqB,
    const short* __restrict__ woB, char* p_ws, unsigned short* __restrict__ out) {
  extern __shared__ __attribute__((aligned(16))) char smem[];
  const int wave = __builtin_amdgcn_readfirstlane(threadIdx.x >> 6);
  const int bid = blockIdx.x;
  const int xcd = bid & 7, idx = bid >> 3;
  STAMP(0);
  unsigned long long code;
  unsigned cline;
  {
    unsigned n0, n1;
    asm volatile("s_getpc_b64 %0\n.Lcode_from:\n\ts_mov_b32 %1, .Lcode_to0-.Lcode_from\n\ts_mov_b32 %2, .Lcode_to1-.Lcode_from"
                 : "=s"(code), "=s"(n0), "=s"(n1));
    const unsigned n = (n0 > n1 ? n0 : n1) >> 7;
    cline = idx * 32 + (threadIdx.x >> 4);
    cline = min(cline, cline - n);
    cline = min(cline, n - 1) * 128;
    if (CT) touch<~0ull>(cline, (const void*)code);
  }
  const int blk = xcd >> 2;
  const int hg = (xcd & 3) * 2 + (idx & 1);
  const int b = idx >> 1;
  const int h0 = hg * 4;
  const int u = wave >> 1, qh = wave & 1;
  const int turn = b % NPH;

  const int w4 = wave & 3;
  unsigned vw[2], vx[2], ta;
  {
    const int lane = lane_id();
    const int c = lane & 15, g = lane >> 4;
#pragma unroll
    for (int e = 0; e < 2; ++e) {
      const int f = g | (e << 2);
      const int ch = (((c >> 1) ^ f) << 1) | (c & 1);
      vw[e] = (unsigned)(g * (HD * 2) + w4 * (DH * 2) + ch * 16);
      const int r = 24 * w4 + 8 * e + (lane >> 3);
      vx[e] = (unsigned)((24 * w4 + (lane >> 3)) * (DM * 2) + (((lane & 7) ^ ((r >> 1) & 7)) << 4));
    }
    const int tl = lane * 4 + w4;
    ta = (unsigned)(((tl >> 4) * 32 + 2 * b + ((tl >> 3) & 1)) * (HD * 2) + (tl & 7) * 128);
    asm volatile("" : "+v"(vw[0]), "+v"(vw[1]), "+v"(vx[0]), "+v"(vx[1]), "+v"(ta));
  }
  __builtin_amdgcn_sched_barrier(0);

  const unsigned lds0 = (unsigned)(uintptr_t)smem;

  const int grp = blk * NB + b;
  auto pc_of = [&](int p) { return (hg + 1 + p) & 7; };
  auto to_mask = [&](int n) { return ((((1u << n) - 1u) * 0x101u) >> ((7 - hg) & 7)) & 0xffu; };
  auto from_mask = [&](int n) { return ((((1u << n) - 1u) * 0x101u) >> ((n - hg) & 7)) & 0xffu; };
  auto front = [&](auto rc) __attribute__((always_inline)) {
    constexpr int ROLE = decltype(rc)::value;
    XS(0);
    const char* __restrict__ x = (const char*)(blk ? xB : xA) + (size_t)b * SQ * DM * 2;
    const char* __restrict__ wq = (const char*)(blk ? wqB : wqA) + h0 * DH * 2;
    auto issue_w = [&](int st, int e0) {
      const int e = ROT && st == 0 ? e0 ^ ((b & 1) * 4) : e0;
      dma16(vw[(e0 >> 1) & 1], wq + (size_t)(st * 32 + 4 * e) * (HD * 2), lds0 + (st & 3) * 32768 + (w4 * 8 + e) * 1024);
    };
    auto issue_x = [&](int j, int e) {
      dma16(vx[e & 1], x + j * 128 + e * 8 * (DM * 2), lds0 + XBASE + (j & 1) * 12288 + (24 * w4 + 8 * e) * 128);
    };
    auto first_requests = [&]() {
    unroll<op_first(ROLE, 0)>([&](auto ic) {
      constexpr Op o = SCH.op[ROLE][decltype(ic)::value];
      if constexpr (o.slot == 0 && o.kind == K_W) issue_w(o.a, o.b);
      if constexpr (o.slot == 0 && o.kind == K_X) issue_x(o.a, o.b);
      if constexpr (o.slot == 0 && o.kind == K_T) touch<((1ull << (4 * o.b)) - 1ull) << (4 * o.a)>(ta, wq);
      static_assert(o.slot || o.kind == K_W || o.kind == K_X || o.kind == K_T);
    });
    __builtin_amdgcn_sched_barrier(0);
    };
    if constexpr (ROLE == 0) first_requests();
    const char* __restrict__ kc = (const char*)(blk ? kB : kA) + ((size_t)b * HQ + h0) * (SQ * DH * 2);
    const char* __restrict__ vc = (const char*)(blk ? vB : vA) + ((size_t)b * HQ + h0) * (SQ * DH * 2);
    const char* __restrict__ wo = (const char*)(blk ? woB : woA) + (size_t)h0 * DH * DM * 2;
    const char* lines2 = p_ws + WS_LINES + (size_t)(grp * 32 + w4) * 256;
    const char* src2 = p_ws + (size_t)(((grp * 8 + hg) * 8 * 4 + w4) * 3072);
    asm volatile("" : "+s"(kc), "+s"(vc), "+s"(wo), "+s"(lines2), "+s"(src2));
    unsigned peek[4] = {0, 0, 0, 0};
    if constexpr (ROLE == 1) {
      peek[0] = speek(wq + (size_t)(w4 * 128) * (HD * 2));
      peek[1] = speek(pick(w4 & 1, vc, kc) + (w4 >> 1) * (4 * SQ * DH * 2 - 128));
      peek[2] = speek(pick(w4 < 2, pick(w4 & 1, x + (SQ * DM * 2 - 128), lines2), wo + (w4 & 1) * (4 * DH * DM * 2 - 128)));
      peek[3] = speek(pick(w4 < 3, (const char*)(out + (size_t)b * SQ * 1024) + w4 * (SQ * 1024 - 64), src2 + 5 * 12288));
      __builtin_amdgcn_sched_barrier(0);
      first_requests();
    }
    const int lane = lane_id();
    const int c = lane & 15, g = lane >> 4, qp = (lane & 15) >> 2, p = lane & 3;
    unsigned vk[2], vv[2];
#pragma unroll
    for (int e = 0; e < 2; ++e) {
      const int row = 8 * w4 + 4 * e + g;
      vk[e] = (unsigned)(row * 256 + ((c ^ (row & 15)) << 4));
    }

    auto piece_src = [&](int i) { return (i >= 12 ? vc : kc) + piece_u(i) * (SQ * DH * 2) + piece_pc(i) * 8192; };
    auto piece_dst = [&](int i, int e) { return lds0 + piece_blk(i) * 8192 + (w4 * 2 + e) * 1024; };
    auto dma_piece = [&](int i, int e) { dma16_nt(i >= 12 ? vv[e] : vk[e], piece_src(i), piece_dst(i, e)); };

    const int fl = qp | ((g & 1) << 2);
    unsigned trw[8];
#pragma unroll
    for (int dt = 0; dt < 8; ++dt) {
      trw[dt] = lds0 + u * 8192 + (8 * g + qp) * 256 + 8 * p + ((dt ^ fl) << 5);
    }
    unsigned xrd[2];
#pragma unroll
    for (int hf = 0; hf < 2; ++hf)
      xrd[hf] = lds0 + XBASE + (qh * 48 + c) * 128 + (((4 * hf + g) ^ ((c >> 1) & 7)) << 4);
    auto read_a = [&](int st, int dt) {
      if (st > 0 && !(st & 1)) {
        if (st & 2) asm volatile("v_add_u32 %0, 0x10000, %0" : "+v"(trw[dt]));
        else asm volatile("v_subrev_u32 %0, 0x10000, %0" : "+v"(trw[dt]));
      }
      const unsigned a0 = trw[dt] + (st & 1) * 32768;
      return cat8(tr_read(a0), tr_read(a0 + 4 * 256));
    };
    auto read_x = [&](int st, int t) { return lds_frag(xrd[st & 1] + ((st >> 1) & 1) * 12288 + t * 16 * 128); };

    f32x4 qacc[8][3];
    bf16x8 af[8], xf[3];
    u32x4 xs[3], stg[18];
    unsigned seen = 0xffffffffu;
    const unsigned tx = (unsigned)(((idx & 1) * 48 + w4 * 12 + c) * (DM * 2) + g * 128);

    auto do_op = [&](auto ic) {
      constexpr Op o = SCH.op[ROLE][decltype(ic)::value];
      if constexpr (o.kind == K_W) issue_w(o.a, o.b);
      if constexpr (o.kind == K_X) issue_x(o.a, o.b);
      if constexpr (o.kind == K_XR) gload_keep(xs[o.b], vx[o.b & 1], x + o.a * 128 + o.b * 8 * (DM * 2));
      if constexpr (o.kind == K_XW) *(lds_u32x4_ptr)(uintptr_t)(lds0 + XBASE + (o.a & 1) * 12288 + (24 * w4 + 8 * o.b) * 128 + lane * 16) = xs[o.b];
      if constexpr (o.kind == K_PD) dma_piece(o.a, o.b);
      if constexpr (o.kind == K_PR && o.ph < 0) gload_nt(stg[o.r], o.a >= 12 ? vv[o.b] : vk[o.b], piece_src(o.a));
      if constexpr (o.kind == K_PR && o.ph >= 0) gload_nt_keep(stg[o.r], o.a >= 12 ? vv[o.b] : vk[o.b], piece_src(o.a));
      if constexpr (o.kind == K_PW) *(lds_u32x4_ptr)(uintptr_t)(piece_dst(o.a, o.b) + lane * 16) = stg[o.r];
      if constexpr (o.kind == K_T) {
        constexpr unsigned long long m = (o.b >= 16 ? ~0ull : (1ull << (4 * o.b)) - 1ull) << (4 * o.a);
        touch<m>(ta, wq);
      }
      if constexpr (o.kind == K_TX) {
        constexpr unsigned long long m = ((o.b >= 4 ? ~0ull : (1ull << (16 * o.b)) - 1ull) << (16 * (o.a & 3))) & 0x0fff0fff0fff0fffull;
        touch<m>(tx, x + (o.a >> 2) * 512);
      }
      if constexpr (o.kind == K_TO && o.a == 1) touch<1ull>(vx[0], out + (size_t)b * SQ * 1024);
      if constexpr (o.kind == K_SEEN)
        asm volatile("s_mov_b64 exec, 0xffff\n\tglobal_load_dword %0, %1, %2 sc1\n\ts_mov_b64 exec, -1"
                     : "+v"(seen) : "v"(lane * 16), "s"(lines2 + hg * 1024) : "memory");
      if constexpr (o.kind == K_PUB) {
        asm volatile("" : "+v"(seen));
        int v = (seen >> 1) < 32u ? 1 << (seen >> 1) : 0;
        v |= __builtin_amdgcn_update_dpp(0, v, 0x111, 0xf, 0xf, true);
        v |= __builtin_amdgcn_update_dpp(0, v, 0x112, 0xf, 0xf, true);
        v |= __builtin_amdgcn_update_dpp(0, v, 0x114, 0xf, 0xf, true);
        v |= __builtin_amdgcn_update_dpp(0, v, 0x118, 0xf, 0xf, true);
        const unsigned j = __builtin_ctz(~(unsigned)__builtin_amdgcn_readlane(v, 15));
        asm volatile("s_mov_b64 exec, 3\n\tglobal_store_dword %0, %1, %2 sc1\n\ts_mov_b64 exec, -1"
                     ::"v"(lane * 128), "v"(2 * j), "s"(lines2 + hg * 1040) : "memory");
      }
    };
    auto mem_ops = [&](auto sc, auto slc) {
      constexpr int s = decltype(sc)::value, sl = decltype(slc)::value, f = op_first(ROLE, s);
      unroll<NPH + 1>([&](auto pc) {
        constexpr int ph = decltype(pc)::value - 1;
        if constexpr (has_ops(ROLE, s, sl, ph)) {
          if (ph < 0 || turn == ph)
            unroll<op_first(ROLE, s + 1) - f>([&](auto ic) {
              constexpr int i = f + decltype(ic)::value;
              if constexpr (SCH.op[ROLE][i].slot == sl && SCH.op[ROLE][i].ph == ph) do_op(IC<i>{});
            });
        }
      });
    };
    auto mem_wait = [&](auto sc) {
      constexpr int s = decltype(sc)::value;
      constexpr int w0 = vm_wait(ROLE, s, 0), w1 = vm_wait(ROLE, s, 1 % NPH), w2 = vm_wait(ROLE, s, 2 % NPH), w3 = vm_wait(ROLE, s, 3 % NPH);
      constexpr int wm = w0 > w1 && w0 > w2 && w0 > w3 ? w0 : w1 > w2 && w1 > w3 ? w1 : w2 > w3 ? w2 : w3;
      if constexpr (wm < 63) wait_vm<wm>();
      if constexpr (w0 < wm) { if (turn == 0) wait_vm<w0>(); }
      if constexpr (w1 < wm) { if (turn == 1) wait_vm<w1>(); }
      if constexpr (NPH > 2 && w2 < wm) { if (turn == 2) wait_vm<w2>(); }
      if constexpr (NPH > 3 && w3 < wm) { if (turn == 3) wait_vm<w3>(); }
    };
#pragma unroll
    for (int i = 0; i < 2 * NE; ++i) asm volatile("" : "=v"(stg[i]));
#pragma unroll
    for (int i = 0; i < 3; ++i) asm volatile("" : "=v"(xs[i]));

    XT(1);
    XT(2);
    if constexpr (ROLE == 0) wait_vm<0>();
    XT(3);
    mem_ops(IC<-1>{}, IC<1>{});
    XT(4);
    if constexpr (ROLE == 1) speek_done(peek);
    XF(1); XF(2); XF(3); XF(4);
    XS(5);
    raw_barrier();
    STAMP(1);
    STAMPW(2);
#pragma unroll
    for (int t = 0; t < 3; ++t) xf[t] = read_x(0, t);
#pragma unroll
    for (int dt = 0; dt < 8; ++dt) af[dt] = read_a(0, dt);

    unsigned l16, klb, vlb, ord0, ow, vo, wa0;
    auto setup = [&](auto dc) {
      constexpr int dt = decltype(dc)::value;
      if constexpr (dt < 5) {
        const int l = lane_id();
        const int c1 = l & 15, g1 = l >> 4, qp1 = c1 >> 2, p1 = l & 3;
        if constexpr (dt == 0) {
          l16 = (unsigned)l * 16;
          ow = lds0 + wave * 4096 + l16;
          ord0 = lds0 + qh * 4096 + (u & 1) * 8192 + l16;
          asm volatile("" : "+v"(l16), "+v"(ow), "+v"(ord0));
        }
        if constexpr (dt == 1) {
          klb = lds0 + u * 8192 + (unsigned)(c1 * 256 + (((g1 >> 1) ^ c1) << 4) + (g1 & 1) * 8);
          asm volatile("" : "+v"(klb));
        }
        if constexpr (dt == 2) {
          vlb = lds0 + u * 8192 + (unsigned)((4 * g1 + qp1) * 256 + 8 * p1 + ((qp1 | ((g1 & 1) << 2)) << 5));
          asm volatile("" : "+v"(vlb));
        }
        if constexpr (dt == 3) {
          const int srow = l >> 3, pos = l & 7;
          vo = (unsigned)((w4 * 64 + srow) * (DM * 2) + (((((pos >> 1) ^ ((srow >> 1) & 3)) << 1) | (pos & 1)) << 4));
          asm volatile("" : "+v"(vo));
        }
        if constexpr (dt == 4) {
          wa0 = lds0 + st_reg(ROLE) + (u & 1) * 16384 + (4 * g1 + qp1) * 128 + 8 * p1 + ((u ^ ((2 * g1 + (qp1 >> 1)) & 3)) << 5);
          asm volatile("" : "+v"(wa0));
        }
      }
    };
    auto group = [&](auto sc, auto dc, bf16x8* an, bf16x8* xn) {
      constexpr int s = decltype(sc)::value, dt = decltype(dc)::value;
#pragma unroll
      for (int t = 0; t < 3; ++t) {
        if constexpr (s == 0) mfma_init(qacc[dt][t], af[dt], xf[t]);
        else mfma_acc(qacc[dt][t], af[dt], xf[t]);
      }
      if constexpr (s > 0) {
        if constexpr (dt < 7) an[dt] = read_a(s + 1, dt);
        if constexpr (dt >= 1 && dt <= 3) xn[dt - 1] = read_x(s + 1, dt - 1);
        if constexpr (dt == 6) an[7] = read_a(s + 1, 7);
      }
      mem_ops(sc, dc);
      if constexpr (s == 14) setup(dc);
      __builtin_amdgcn_sched_barrier(0);
    };
    auto step = [&](auto ic) {
      constexpr int s = decltype(ic)::value;
      if constexpr (s > 0) mem_wait(ic);
      raw_barrier();
      STAMP(3 + s);
      bf16x8 xn[3], an[8];
      group(ic, IC<0>{}, an, xn); group(ic, IC<1>{}, an, xn); group(ic, IC<2>{}, an, xn); group(ic, IC<3>{}, an, xn);
      group(ic, IC<4>{}, an, xn); group(ic, IC<5>{}, an, xn); group(ic, IC<6>{}, an, xn); group(ic, IC<7>{}, an, xn);
      if constexpr (s == 0) {
        mem_wait(ic);
        raw_barrier();
#pragma unroll
        for (int t = 0; t < 3; ++t) xn[t] = read_x(1, t);
#pragma unroll
        for (int dt = 0; dt < 8; ++dt) an[dt] = read_a(1, dt);
      }
#pragma unroll
      for (int t = 0; t < 3; ++t) xf[t] = xn[t];
#pragma unroll
      for (int dt = 0; dt < 8; ++dt) af[dt] = an[dt];
    };
    step(IC<0>{}); step(IC<1>{}); step(IC<2>{}); step(IC<3>{}); step(IC<4>{}); step(IC<5>{}); step(IC<6>{}); step(IC<7>{});
    step(IC<8>{}); step(IC<9>{}); step(IC<10>{}); step(IC<11>{});
    {
      const int l = lane_id();
#pragma unroll
      for (int e = 0; e < 2; ++e) {
        const int row = 8 * w4 + 4 * e + (l >> 4);
        vv[e] = (unsigned)(row * 256 + ((((((l & 15) >> 1) ^ (row & 7)) << 1) | (l & 1)) << 4));
      }
    }
    step(IC<12>{}); step(IC<13>{}); step(IC<14>{});

    mem_wait(IC<15>{});
    raw_barrier();
    STAMP(18);
    u32x4 qf[4][3];
    bf16x8 kf[6][4];
    unsigned kb[2];
    auto read_k = [&](auto jc) {
      constexpr int j = decltype(jc)::value;
      if constexpr (j < 24) {
        constexpr int s = j / 6, kt = j % 6;
        constexpr unsigned off = (kt & 2) * 16384u + (kt & 1) * 4096u;
        if constexpr (kt == 0 || kt == 4) {
          kb[0] = xbase(klb, (s << 6) | (kt << 14));
          kb[1] = xbase(klb, (s << 6) | 32 | (kt << 14));
        }
        kf[kt][s] = cat8(lds_half(kb[0] + off), lds_half(kb[1] + off));
      }
    };
    auto q_cvt = [&](int dt) {
#pragma unroll
      for (int t = 0; t < 3; ++t) {
        qf[dt >> 1][t][(dt & 1) * 2] = pack2(qacc[dt][t][0], qacc[dt][t][1]);
        qf[dt >> 1][t][(dt & 1) * 2 + 1] = pack2(qacc[dt][t][2], qacc[dt][t][3]);
      }
    };
    unroll<8>([&](auto dc) {
      constexpr int dt = decltype(dc)::value;
#pragma unroll
      for (int t = 0; t < 3; ++t) qacc[dt][t] = MFMA16(af[dt], xf[t], qacc[dt][t]);
      if constexpr (dt >= 2) q_cvt(dt - 2);
      if constexpr (dt == 7) q_cvt(6);
      read_k(dc);
      mem_ops(IC<15>{}, dc);
      __builtin_amdgcn_sched_barrier(0);
    });

    const f32x4 zero4 = {0.f, 0.f, 0.f, 0.f};
    const float cexp = 0.08838834764831845f * 1.4426950408889634f;
    f32x4 sacc[6][3], oacc[8][3], lsum[3];
    float mx[3], ex[3][24], rinv[3];
    u32x4 pf[3][3];
    bf16x8 vf[3][8], of[2][2][4][3];
    auto s_mm = [&](auto tc, auto jc) {
      constexpr int t = decltype(tc)::value, j = decltype(jc)::value, s = j / 6, kt = j % 6;
      sacc[kt][t] = MFMA16(kf[kt][s], __builtin_bit_cast(bf16x8, qf[s][t]), s == 0 ? zero4 : sacc[kt][t]);
    };
    auto m_op = [&](auto tc, auto qc) {
      constexpr int t = decltype(tc)::value, q = decltype(qc)::value;
      auto sv = [&](int j) { return sacc[j >> 2][t][j & 3]; };
      if constexpr (q < 4) {
#pragma unroll
        for (int i = 3 * q; i < 3 * q + 3; ++i) {
          if (i == 0) mx[t] = max3(sv(0), sv(1), sv(2));
          else if (i == 11) mx[t] = max2(mx[t], sv(23));
          else mx[t] = max3(mx[t], sv(2 * i + 1), sv(2 * i + 2));
        }
      } else if constexpr (q == 4) {
        auto s = __builtin_amdgcn_permlane16_swap(__float_as_uint(mx[t]), __float_as_uint(mx[t]), false, false);
        mx[t] = max2(__uint_as_float(s[0]), __uint_as_float(s[1]));
      } else if constexpr (q == 5) {
        auto s = __builtin_amdgcn_permlane32_swap(__float_as_uint(mx[t]), __float_as_uint(mx[t]), false, false);
        mx[t] = max2(__uint_as_float(s[0]), __uint_as_float(s[1])) * -cexp;
      }
#pragma unroll
      for (int j2 = 0; j2 < 12; ++j2) {
        const int st = 6 + (j2 * 15) / 11;
        if (q == st + 2) pf[j2 >> 2][t][j2 & 3] = pack2(ex[t][2 * j2], ex[t][2 * j2 + 1]);
        if (q == st + 1) {
          ex[t][2 * j2] = __builtin_amdgcn_exp2f(ex[t][2 * j2]);
          ex[t][2 * j2 + 1] = __builtin_amdgcn_exp2f(ex[t][2 * j2 + 1]);
        }
        if (q == st) {
          ex[t][2 * j2] = fmaf(sv(2 * j2), cexp, mx[t]);
          ex[t][2 * j2 + 1] = fmaf(sv(2 * j2 + 1), cexp, mx[t]);
        }
      }
    };
    unsigned vb[8];
    auto read_v = [&](auto ic) {
      constexpr int i = decltype(ic)::value, s = i >> 3, dt = i & 7;
      if constexpr (s != 1) vb[dt] = xbase(vlb, (dt << 5) | (s ? 131072 : 65536));
      const unsigned a = vb[dt] + (s == 1 ? 32768u : 0u);
      vf[s][dt] = cat8(tr_read(a), tr_read(a + 16 * 256));
    };
    u32x4 ones = {0x3f803f80u, 0x3f803f80u, 0x3f803f80u, 0x3f803f80u};
    auto p_mm = [&](auto tc, auto jc) {
      constexpr int t = decltype(tc)::value, j = decltype(jc)::value, s = j / 9, dt = j % 9;
      const bf16x8 pb = __builtin_bit_cast(bf16x8, pf[s][t]);
      if constexpr (dt == 8) lsum[t] = MFMA16(__builtin_bit_cast(bf16x8, ones), pb, j < 9 ? zero4 : lsum[t]);
      else oacc[dt][t] = MFMA16(vf[s][dt], pb, j < 9 ? zero4 : oacc[dt][t]);
    };
    unsigned owb, orb[2];
    auto o_op = [&](auto tc, auto qc) {
      constexpr int t = decltype(tc)::value, q = decltype(qc)::value;
      if constexpr (q == 0) {
        rinv[t] = __builtin_amdgcn_rcpf(lsum[t][0]);
        owb = xbase(ow, o_reg(t));
      } else if constexpr (q <= 12) {
        constexpr int s = (q - 1) / 3, k = (q - 1) % 3;
        if constexpr (k < 2) {
#pragma unroll
          for (int r = 0; r < 4; ++r) oacc[2 * s + k][t][r] = t < 2 ? mul1(oacc[2 * s + k][t][r], rinv[t]) : oacc[2 * s + k][t][r] * rinv[t];
        } else {
          u32x4 r;
          r[0] = pack2(oacc[2 * s][t][0], oacc[2 * s][t][1]);
          r[1] = pack2(oacc[2 * s][t][2], oacc[2 * s][t][3]);
          r[2] = pack2(oacc[2 * s + 1][t][0], oacc[2 * s + 1][t][1]);
          r[3] = pack2(oacc[2 * s + 1][t][2], oacc[2 * s + 1][t][3]);
          *(lds_u32x4_ptr)(uintptr_t)(owb + s * 1024) = r;
          of[ROLE][0][s][t] = __builtin_bit_cast(bf16x8, r);
        }
      }
    };
    auto read_o = [&](auto tc, auto ic) {
      constexpr int t = decltype(tc)::value, i = decltype(ic)::value;
      constexpr int half = i < 4 ? ROLE : 1 - ROLE, hh = i < 4 ? 1 : (i - 4) >> 2, s = i & 3;
      if constexpr (i == 0 || i == 4) orb[hh] = xbase(ord0, (hh * 8192) | o_reg(t));
      of[half][hh][s][t] = lds_frag(orb[hh] + (half * 16384 + s * 1024));
    };
    auto issue_wo = [&](int st, int e) {
      dma16(vo, wo + (size_t)((st & 1) * 256 + e * 8) * (DM * 2) + pc_of(st >> 1) * 128, lds0 + st_reg(st) + (w4 * 64 + e * 8) * 128);
    };
    const char* lines = lines2 + ROLE * 128;
    const char* src = src2 + ROLE * 1536;
    const unsigned others = 0xffu & ~(1u << hg);
    unsigned tag = 0, mk = 0, pl = 0;
    auto signal = [&](unsigned v, unsigned m) {
      unsigned a;
      asm volatile("v_lshlrev_b32 %0, 6, %1\n\ts_mov_b32 exec_lo, %4\n\ts_mov_b32 exec_hi, 0\n\tglobal_store_dword %0, %2, %3 sc1\n\ts_mov_b64 exec, -1"
                   : "=&v"(a) : "v"(l16), "v"(v), "s"(lines + hg * 16), "s"(m) : "memory");
    };
    auto poll = [&](unsigned& f) {
      asm volatile("s_mov_b64 exec, 0xff\n\tglobal_load_dword %0, %1, %2 sc1\n\ts_mov_b64 exec, -1"
                   : "+v"(f) : "v"(l16), "s"(lines + hg * 1024) : "memory");
    };
    auto marks = [&](unsigned& m) {
      unsigned a;
      asm volatile("v_mul_u32_u24 %1, %4, %2\n\ts_mov_b64 exec, 0xff\n\tglobal_load_dword %0, %1, %3 sc1\n\ts_mov_b64 exec, -1"
                   : "+v"(m), "=&v"(a) : "v"(l16), "s"(lines), "n"(65) : "memory");
    };
    auto heard = [&](unsigned& f) {
      asm volatile("" : "+v"(f));
      const unsigned got = (unsigned)__builtin_amdgcn_ballot_w64((f ^ __builtin_amdgcn_readlane(f, hg)) < 2u);
      return (got & 0xffu) == 0xffu;
    };
    auto ready = [&](unsigned& f, unsigned need) {
      asm volatile("" : "+v"(f));
      const unsigned got = (unsigned)__builtin_amdgcn_ballot_w64(f == __builtin_amdgcn_readlane(f, hg) + 1u);
      return (got & need) == need;
    };
    auto retag = [&](unsigned& m) {
      asm volatile("" : "+v"(m));
      const unsigned bad = (unsigned)__builtin_amdgcn_ballot_w64(m != tag) & others;
      tag = m;
      if (bad) signal(tag, bad);
    };
    auto hook = [&](auto pc, auto jc) {
      constexpr int P = decltype(pc)::value, j = decltype(jc)::value, q = (j - CLJ) / 2 * 2 + ROLE;
      if constexpr (P == TOP && j == TOJ) {
        const int tw = (lane_id() & 31) * 8 + wave;
        touch<0xffffffffull>((unsigned)(((tw >> 4) * 32 + 2 * b + ((tw >> 3) & 1)) * (DM * 2) + (tw & 7) * 128), wo);
        if constexpr (WT) {
          const unsigned own = (unsigned)(((grp * 8 + hg) * 8 + wave) * 12288), far = (unsigned)(((grp * 8 + pc_of(wave)) * 8 + ((wave + 1) & 7)) * 12288);
          touch_dev<3ull>(lane_id() & 1 ? far : own, p_ws);
        }
      }
      if constexpr (P == CLR && j >= CLJ && !((j - CLJ) & 1) && q < 9) store_empty<(q % 3) * 1024>(l16, src2 + (5 + q / 3) * 12288);
      if constexpr (P == STP && j >= STJ && !((j - STJ) & 1) && j < STJ + 16) issue_wo(ROLE, (j - STJ) / 2);
      if constexpr (ROLE == 1 && P == 5 && j == 23) marks(tag);
    };

    unroll<24>([&](auto jc) {
      constexpr int j = decltype(jc)::value;
      s_mm(IC<0>{}, jc);
      if constexpr (j == 1) q_cvt(7);
      read_k(IC<j + 8>{});
      __builtin_amdgcn_sched_barrier(0);
    });
    mem_wait(IC<16>{});
    raw_barrier();
    STAMP(19);
    unroll<24>([&](auto jc) {
      constexpr int j = decltype(jc)::value;
      s_mm(IC<1>{}, jc);
      if constexpr (j == 0 || j == 4) { owb = piece_dst(j < 4 ? 12 + 2 * ROLE : 21 + ROLE, 0) + l16; asm volatile("" : "+v"(owb)); }
      if constexpr (j < 6) *(lds_u32x4_ptr)(uintptr_t)(owb + (j & 2) * 4096 + (j & 1) * 1024) = stg[12 + j];
      hook(IC<2>{}, jc);
      m_op(IC<0>{}, jc);
      __builtin_amdgcn_sched_barrier(0);
    });
    raw_barrier();
    STAMP(20);
    unroll<24>([&](auto jc) {
      constexpr int j = decltype(jc)::value;
      s_mm(IC<2>{}, jc);
      m_op(IC<1>{}, jc);
      if constexpr (j >= 8 && j < 24 && !(j & 1)) read_v(IC<(j - 8) / 2>{});
      hook(IC<3>{}, jc);
      __builtin_amdgcn_sched_barrier(0);
    });
    wait_vm<(TOP <= 3) * (1 + WT)>();
    raw_barrier();
    STAMP(21);
    unroll<27>([&](auto jc) {
      constexpr int j = decltype(jc)::value;
      p_mm(IC<0>{}, jc);
      if constexpr (j < 16) read_v(IC<j + 8>{});
      if constexpr (j >= 2 && j < 26) m_op(IC<2>{}, IC<j - 2>{});
      hook(IC<4>{}, jc);
      __builtin_amdgcn_sched_barrier(0);
    });
    raw_barrier();
    STAMP(22);
    unroll<27>([&](auto jc) {
      constexpr int j = decltype(jc)::value;
      p_mm(IC<1>{}, jc);
      if constexpr (j & 1) o_op(IC<0>{}, IC<j / 2>{});
      hook(IC<5>{}, jc);
      __builtin_amdgcn_sched_barrier(0);
    });
    raw_barrier();
    STAMP(23);
    unroll<27>([&](auto jc) {
      constexpr int j = decltype(jc)::value;
      p_mm(IC<2>{}, jc);
      if constexpr (j & 1) o_op(IC<1>{}, IC<j / 2>{});
      hook(IC<6>{}, jc);
      if constexpr (j >= 2 && j < 26 && !(j & 1)) read_o(IC<0>{}, IC<(j - 2) / 2>{});
      __builtin_amdgcn_sched_barrier(0);
    });
    wait_vm<(CLR == 6 ? 5 - ROLE : 0)>();
    raw_barrier();
    STAMP(24);

    unsigned wa[2];
    wa[0] = wa0;
    wa[1] = wa[0] ^ 16384u;
    asm volatile("" : "+v"(wa[0]), "+v"(wa[1]));
    bf16x8 wf[4];
    auto read_wo = [&](auto fc) {
      constexpr int f = decltype(fc)::value, n = f >> 3, hh = (f >> 2) & 1, ks = f & 3;
      if constexpr (n < 16) {
        if constexpr (n > 0 && ks == 0)
          asm volatile("v_add_u32 %0, %1, %0" : "+v"(wa[hh]) : "n"(st_reg(n < 2 ? n ^ ROLE : n) - st_reg(n - 1 < 2 ? (n - 1) ^ ROLE : n - 1)));
        wf[f & 3] = cat8(tr_read(wa[hh] + ks * 4096), tr_read(wa[hh] + ks * 4096 + 16 * 128));
      }
    };
    unroll<13>([&](auto jc) {
      constexpr int j = decltype(jc)::value;
      if constexpr (j < 12) read_o(IC<1>{}, jc);
      o_op(IC<2>{}, jc);
      if constexpr (ROLE == 0 && j < 8) issue_wo(2, j);
      if constexpr (CLR != 6 && ROLE == 1 && j == 0) { asm volatile("" : "+v"(tag)); signal(tag, others); }
      if constexpr (j >= 10) read_wo(IC<j - 10>{});
      __builtin_amdgcn_sched_barrier(0);
    });
    raw_barrier();
    STAMP(25);

    f32x4 acc[2][3];
    u32x4 in4[7];
    u32x2 in2[7];
    unsigned early = 0;
    const unsigned l8 = l16 >> 1;
    auto load_in = [&](int k) {
      asm volatile("global_load_dwordx4 %0, %2, %4 sc1\n\tglobal_load_dwordx2 %1, %3, %4 offset:1024 sc1"
                   : "=&v"(in4[k - 1]), "=&v"(in2[k - 1]) : "v"(l16), "v"(l8), "s"(src + k * 12288) : "memory");
    };
    auto oint = [&](auto nc) {
      constexpr int n = decltype(nc)::value;
      constexpr int half = (n < 2 ? n ^ ROLE : n) & 1, pb = (n >> 1) & 1;
      unroll<24>([&](auto ic) {
        constexpr int i = decltype(ic)::value, f = i / 3, t = i % 3;
        if constexpr (!(n & 1) && f == 0) mfma_init(acc[pb][t], wf[f & 3], of[half][f >> 2][f & 3][t]);
        else mfma_acc(acc[pb][t], wf[f & 3], of[half][f >> 2][f & 3][t]);
        if constexpr (t == 0) read_wo(IC<n * 8 + f + 3>{});
        if constexpr (n == 0 && i < 12) read_o(IC<2>{}, ic);
        if constexpr (ROLE == 0 && t == 0 && n + 3 <= 15) issue_wo(n + 3, f);
        if constexpr (CLR == 6 && ROLE == 1 && n == 1 && i == 1) { asm volatile("" : "+v"(tag)); signal(tag, others); }
        if constexpr (ROLE == 1 && n == 3 && i == 2) poll(pl);
        if constexpr (n == 3 && i == 1) marks(mk);
        if constexpr ((n == 6 || n == 8 || n == 10) && i == 1) {
          if constexpr (ROLE == 1) wait_vm<0>();
          if constexpr (n == 6) {
            if constexpr (ROLE == 1) retag(mk);
            asm volatile("" : "+v"(mk));
            tag = mk + 1u;
          }
          signal(tag, n == 6 ? to_mask(2) : 1u << pc_of(n / 2 - 2));
        }
        if constexpr (n >= 2 && !(n & 1) && i == 4) {
          constexpr int p = n / 2 - 1;
          const char* dst = p_ws + (size_t)((((grp * 8 + pc_of(p)) * 8 + p + 1) * 4 + w4) * 3072 + ROLE * 1536);
          u32x4 r;
          u32x2 r2;
          r[0] = pack2(acc[1 - pb][0][0], acc[1 - pb][0][1]); r[1] = pack2(acc[1 - pb][0][2], acc[1 - pb][0][3]);
          r[2] = pack2(acc[1 - pb][1][0], acc[1 - pb][1][1]); r[3] = pack2(acc[1 - pb][1][2], acc[1 - pb][1][3]);
          r2[0] = pack2(acc[1 - pb][2][0], acc[1 - pb][2][1]); r2[1] = pack2(acc[1 - pb][2][2], acc[1 - pb][2][3]);
          asm volatile("global_store_dwordx4 %0, %2, %4 sc1\n\tglobal_store_dwordx2 %1, %3, %4 offset:1024 sc1\n\ts_nop 1"
                       ::"v"(l16), "v"(l8), "v"(r), "v"(r2), "s"(dst) : "memory");
        }
        if constexpr (n == 12 && i == 23) poll(pl);
        if constexpr (n == 15 && i == 0) {
          wait_vm<2>();
          early = __builtin_amdgcn_readfirstlane(ready(pl, from_mask(4)) ? 1u : 0u);
        }
        if constexpr (n == 15 && i >= 1 && i <= 6) load_in(i);
        if constexpr (n == 15 && i == 16) load_in(7);
        __builtin_amdgcn_sched_barrier(0);
      });
      if constexpr (ROLE == 1 && n == 0) wait_vm<0>();
      if constexpr (ROLE == 1 && n == 5) {
        wait_vm<2>();
        if (!heard(pl)) {
#pragma unroll 1
          for (int it = 0; it < SPIN_CAP; ++it) {
            unsigned m = tag;
            poll(pl);
            marks(m);
            wait_vm<0>();
            retag(m);
            if (heard(pl)) break;
            __builtin_amdgcn_s_sleep(1);
          }
        }
      }
      if constexpr (n < 14) {
        if constexpr (ROLE == 0) wait_vm<ops0(n) + (n > 0 ? after0(n - 1) : 0)>();
        raw_barrier();
        if constexpr (n & 1) { STAMP(26 + n / 2); }
      }
    };
    unroll<16>(oint);
    asm volatile("s_nop 7\n\ts_nop 3" ::: "memory");
    STAMPW(33);

    auto add_in = [&](int k) {
      asm volatile("" : "+v"(in4[k - 1]), "+v"(in2[k - 1]));
#pragma unroll
      for (int e = 0; e < 6; ++e) {
        const unsigned w = e < 4 ? in4[k - 1][e & 3] : in2[k - 1][e & 1];
        acc[1][e >> 1][2 * (e & 1)] += __uint_as_float(w << 16);
        acc[1][e >> 1][2 * (e & 1) + 1] += __uint_as_float(w & 0xffff0000u);
      }
      asm volatile("" : "+v"(acc[1][0]), "+v"(acc[1][1]), "+v"(acc[1][2]));
    };
    auto whole = [&]() {
      bool ok = true;
#pragma unroll
      for (int k = 4; k < 7; ++k) {
        asm volatile("" : "+v"(in4[k]), "+v"(in2[k]));
        ok = ok && in4[k][0] != EMPTY && in2[k][0] != EMPTY;
      }
      return __builtin_amdgcn_ballot_w64(!ok) == 0;
    };
    wait_vm<6>();
    if (!early) {
#pragma unroll 1
      for (int it = 0; it < SPIN_CAP; ++it) {
        poll(pl);
        wait_vm<0>();
        if (ready(pl, from_mask(4))) break;
        __builtin_amdgcn_s_sleep(1);
      }
      load_in(1); load_in(2); load_in(3); load_in(4);
      wait_vm<0>();
    }
    add_in(1); add_in(2); add_in(3); add_in(4);
    STAMPW(34);
    wait_vm<0>();
    if (!whole()) {
#pragma unroll 1
      for (int it = 0; it < SPIN_CAP; ++it) {
        load_in(5); load_in(6); load_in(7);
        wait_vm<0>();
        if (whole()) break;
      }
    }
    add_in(5); add_in(6); add_in(7);
    STAMPW(35);
    {
      const int lane2 = lane_id();
      unsigned short* o = out + (size_t)(b * SQ + qh * 48 + (lane2 & 15)) * 1024 + blk * 512 + hg * 64 + u * 16 + 4 * (lane2 >> 4);
#pragma unroll
      for (int t = 0; t < 3; ++t) {
        u32x2 r;
        r[0] = pack2(acc[1][t][0], acc[1][t][1]);
        r[1] = pack2(acc[1][t][2], acc[1][t][3]);
        *(u32x2*)(o + t * 16 * 1024) = r;
      }
    }
    STAMPW(36);
    STAMPEND(37);
    code_end<ROLE>();
  };
  if (wave >= 4) {
    front(IC<1>{});
    __builtin_amdgcn_endpgm();
  }
  front(IC<0>{});
}

}

extern "C" void kernel_launch(void* const* d_in, const int* in_sizes, int n_in, void* d_out, int out_size,
                              void* d_ws, size_t ws_size, hipStream_t stream) {
  const short* xA = (const short*)d_in[0];
  const short* kA = (const short*)d_in[1];
  const short* vA = (const short*)d_in[2];
  const short* wqA = (const short*)d_in[3];
  const short* woA = (const short*)d_in[4];
  const short* xB = (const short*)d_in[5];
  const short* kB = (const short*)d_in[6];
  const short* vB = (const short*)d_in[7];
  const short* wqB = (const short*)d_in[8];
  const short* woB = (const short*)d_in[9];
  (void)hipFuncSetAttribute((const void*)fused_kernel, hipFuncAttributeMaxDynamicSharedMemorySize, K1_LDS);
  hipLaunchKernelGGL(fused_kernel, dim3(256), dim3(512), K1_LDS, stream, xA, kA, vA, wqA, woA, xB, kB, vB, wqB,
                     woB, (char*)d_ws, (unsigned short*)d_out);
}
```
